```python
import torch

def _softmax(x, axis=-1):
    return torch.softmax(x, dim=axis)
def _gelu(x):
    return torch.nn.functional.gelu(x, approximate="tanh")
def _silu(x):
    return torch.nn.functional.silu(x)
def _sigmoid(x):
    return torch.sigmoid(x)
def _layernorm(x, axis=-1, eps=1e-5):
    m = x.mean(dim=axis, keepdim=True)
    v = x.var(dim=axis, keepdim=True, unbiased=False)
    return (x - m) / torch.sqrt(v + eps)
def _scatter_add(tgt, idx, src, axis=0):
    return tgt.clone().index_add_(axis, idx, src)
def _segment_sum(src, seg, n):
    out = torch.zeros((n,) + src.shape[1:], dtype=src.dtype)
    return out.index_add_(0, seg, src)
def _pad(x, pw):
    flat = tuple(v for pair in reversed(pw) for v in pair)
    return torch.nn.functional.pad(x, flat)

kernel_name = 'recurrent_scan__linear_kernel_hedgehog__dense_activation_gelu__prompt'

def setup_inputs():

    _w = (lambda a: (lambda t: t.to(torch.bfloat16) if t.is_floating_point() else t)(torch.as_tensor(a)))
    def _A_setup():
        B, S, D, H = 1, 2048, 576, 32
        Dh = D // H
        return {
            "x": _w((lambda *s: torch.randn(*s, dtype=torch.bfloat16) * 0.05)(B, S, D)),
            "wqkv": _w((lambda *s: torch.randn(*s, dtype=torch.bfloat16) * 0.4)(D, 3 * D)),
            "w_phi": _w((lambda *s: torch.randn(*s, dtype=torch.bfloat16) * 0.4)(H, Dh, Dh) * (Dh ** -0.5)),
            "wo": _w((lambda *s: torch.randn(*s, dtype=torch.bfloat16) * 0.2)(D, D)),
        }
    _A_in = _A_setup()

    def _B_setup():
        B, S, D, F = 1, 2048, 576, 4608
        return {
            "x": _w((lambda *s: torch.randn(*s, dtype=torch.bfloat16) * 0.05)(B, S, D)),
            "w1": _w((lambda *s: torch.randn(*s, dtype=torch.bfloat16) * 0.4)(D, F) * (D ** -0.5)),
            "b1": _w((lambda *s: torch.randn(*s, dtype=torch.bfloat16) * 12.8)(F) * 0.01),
            "w2": _w((lambda *s: torch.randn(*s, dtype=torch.bfloat16) * 0.1)(F, D) * (F ** -0.5)),
            "b2": _w((lambda *s: torch.randn(*s, dtype=torch.bfloat16) * 0.8)(D) * 0.01),
        }
    _B_in = _B_setup()

    return {
        "A_x": _A_in["x"],
        "A_wqkv": _A_in["wqkv"],
        "A_w_phi": _A_in["w_phi"],
        "A_wo": _A_in["wo"],
        "B_x": _B_in["x"],
        "B_w1": _B_in["w1"],
        "B_b1": _B_in["b1"],
        "B_w2": _B_in["w2"],
        "B_b2": _B_in["b2"],
    }

def reference(A_x, A_wqkv, A_w_phi, A_wo, B_x, B_w1, B_b1, B_w2, B_b2):
    def _A_ref(x, wqkv, w_phi, wo):
        B, S, D = x.shape
        H = 32; Dh = D // H
        qkv = (lambda x, *d: x.permute(*d))((x @ wqkv).reshape(B, S, 3, H, Dh), 2, 0, 3, 1, 4)
        q, k, v = qkv[0], qkv[1], qkv[2]
        pq = torch.einsum("bhsd,hdf->bhsf", q, w_phi)
        pk = torch.einsum("bhsd,hdf->bhsf", k, w_phi)
        phi_q = torch.concatenate([_softmax(pq, axis=-1), _softmax(-pq, axis=-1)], axis=-1)
        phi_k = torch.concatenate([_softmax(pk, axis=-1), _softmax(-pk, axis=-1)], axis=-1)
        kv = torch.einsum("bhsf,bhsd->bhfd", phi_k, v)
        z  = torch.einsum("bhsf->bhf", phi_k)
        num = torch.einsum("bhqf,bhfd->bhqd", phi_q, kv)
        den = torch.einsum("bhqf,bhf->bhq", phi_q, z)[..., None] + 1e-6
        o = num / den
        return (lambda x, *d: x.permute(*d))(o, 0, 2, 1, 3).reshape(B, S, D) @ wo

    def _B_ref(x, w1, b1, w2, b2):
        h = torch.einsum("bsd,df->bsf", x, w1) + b1
        h = _gelu(h)
        return torch.einsum("bsf,fd->bsd", h, w2) + b2

    x = A_x
    xn = _layernorm(x, axis=-1)
    s = _A_ref(xn, A_wqkv, A_w_phi, A_wo)
    g = _silu(_B_ref(xn, B_w1, B_b1, B_w2, B_b2))
    return x + s * g
```

```cpp
#include <hip/hip_runtime.h>
#include <hip/hip_bf16.h>
#include <stdint.h>


typedef __attribute__((ext_vector_type(8))) __bf16 bf16x8;
typedef __attribute__((ext_vector_type(4))) __bf16 bf16x4;
typedef __attribute__((ext_vector_type(2))) __bf16 bf16x2;
typedef __attribute__((ext_vector_type(4))) short s16x4;
typedef __attribute__((ext_vector_type(8))) short s16x8;
typedef __attribute__((ext_vector_type(16))) float f32x16;
typedef __attribute__((ext_vector_type(4))) float f32x4;
typedef __attribute__((ext_vector_type(2))) float f32x2;
typedef __attribute__((ext_vector_type(4))) unsigned u32x4;
typedef __attribute__((ext_vector_type(2))) unsigned u32x2;
typedef unsigned short u16;
typedef __attribute__((address_space(3))) s16x4* lds_s16x4_p;
typedef __attribute__((address_space(3))) void* lds_void_p;
typedef __attribute__((address_space(1))) const void* g_void_p;

#define DI __device__ __forceinline__

constexpr int S_ = 2048, D_ = 576, H_ = 32, DH = 18, F_ = 4608;
constexpr int NCH = 8;
constexpr int FC = F_ / NCH;
constexpr int NMT = S_ / 64;

constexpr size_t WS_CNT  = 0;
constexpr size_t WS_KVP  = 1024;
constexpr size_t WS_KVR  = WS_KVP + (size_t)NMT * 32 * 720 * 4;
constexpr size_t WS_PHIQ = WS_KVR + (size_t)32 * 720 * 4;
constexpr size_t WS_WOT  = WS_PHIQ + (size_t)S_ * 32 * 40 * 2 + 256;
constexpr size_t WS_PART = WS_WOT + (size_t)D_ * D_ * 2;

constexpr int L_W1   = 0;
constexpr int L_W2   = 73728;
constexpr int L_QST  = 0;
constexpr int L_XN   = 86016;
constexpr int L_PHIK = 73728;
constexpr int L_VT   = L_PHIK + 4 * 36 * 128;
constexpr int L_QKV  = 114688;
constexpr int L_X    = 147456;
constexpr int L_B1   = 155648;
constexpr int L_WPHI = L_B1 + 576 * 4;
constexpr int LDS1   = 163840;

DI float bf2f(u16 v) { return __uint_as_float(((unsigned)v) << 16); }
DI float rbf(float f) { return (float)((__bf16)f); }
DI u16 f2bf(float f) { __bf16 b = (__bf16)f; return __builtin_bit_cast(u16, b); }
DI unsigned pack2(float a, float b) {
  f32x2 v = {a, b};
  bf16x2 p = __builtin_convertvector(v, bf16x2);
  return __builtin_bit_cast(unsigned, p);
}
DI float lo_bf(unsigned u) { return __uint_as_float(u << 16); }
DI float hi_bf(unsigned u) { return __uint_as_float(u & 0xffff0000u); }

DI void waitv0()  { asm volatile("s_waitcnt vmcnt(0)" ::: "memory"); }
DI void bar() {
  asm volatile("s_waitcnt lgkmcnt(0)" ::: "memory");
  __builtin_amdgcn_s_barrier();
  asm volatile("" ::: "memory");
}
#define WAITV(n) asm volatile("s_waitcnt vmcnt(" #n ")" ::: "memory")

DI s16x4 tr_read(const char* base, int off) {
  return __builtin_amdgcn_ds_read_tr16_b64_v4i16((lds_s16x4_p)(base + off));
}
DI bf16x8 cat8(s16x4 a, s16x4 b) {
  s16x8 v = __builtin_shufflevector(a, b, 0, 1, 2, 3, 4, 5, 6, 7);
  return __builtin_bit_cast(bf16x8, v);
}
DI void glds16(const void* g, char* l) {
  __builtin_amdgcn_global_load_lds((g_void_p)g, (lds_void_p)l, 16, 0, 0);
}
#define MFMA32(a, b, c) __builtin_amdgcn_mfma_f32_32x32x16_bf16((a), (b), (c), 0, 0, 0)
#define MFMA16(a, b, c) __builtin_amdgcn_mfma_f32_16x16x32_bf16((a), (b), (c), 0, 0, 0)

DI float gelu_tanh(float x) {
  float u = x * (0.7978845608028654f + 0.0356774081363001f * x * x);
  float e = __expf(-2.0f * u);
  return x * __builtin_amdgcn_rcpf(1.0f + e);
}

__global__ __launch_bounds__(512, 2)
void k1(const u16* __restrict__ x, const u16* __restrict__ wqkv, const u16* __restrict__ wphi,
        const u16* __restrict__ wo, const u16* __restrict__ w1, const u16* __restrict__ b1,
        const u16* __restrict__ w2, char* __restrict__ ws) {
  extern __shared__ __attribute__((aligned(16))) char smem[];
  const int tid = threadIdx.x, lane = tid & 63;
  const int wid = __builtin_amdgcn_readfirstlane(tid >> 6);
  const int c = blockIdx.x & 7, mt64 = blockIdx.x >> 3, m0 = mt64 * 64;
  const int r = lane & 31, h = lane >> 5;
  const int blk = (lane >> 4) & 1, q = (lane & 15) >> 2, p = lane & 3;
  const int lpar = (wid >> 1) & 1, lhf = wid & 1;

  int goff[7];
  if (wid >= 4) {
#pragma unroll
    for (int e = 0; e < 7; ++e) {
      int pp = ((wid - 4) * 7 + e) * 64 + lane, row = pp / 28, ch = pp % 28;
      if (ch == 27) ch = 26;
      goff[e] = row * 1728 + (ch / 9) * 576 + c * 72 + (ch % 9) * 8;
    }
  }
  auto issue_qkv = [&](int kc) {
    const u16* g = wqkv + (size_t)kc * 64 * 1728;
    char* l = smem + L_QST + (kc % 3) * 28672 + (wid - 4) * 7 * 1024;
#pragma unroll
    for (int e = 0; e < 7; ++e) glds16(g + goff[e], l + e * 1024);
  };

  {
    const int tok = tid >> 3, part = tid & 7;
    const u16* xr = x + (size_t)(m0 + tok) * D_ + part * 8;
    u32x4 raw[9];
#pragma unroll
    for (int i = 0; i < 9; ++i) raw[i] = *(const u32x4*)(xr + 64 * i);
    float sum = 0.f;
#pragma unroll
    for (int i = 0; i < 9; ++i)
#pragma unroll
      for (int j = 0; j < 4; ++j) sum += lo_bf(raw[i][j]) + hi_bf(raw[i][j]);
    if (wid >= 4) { issue_qkv(0); issue_qkv(1); }
    sum += __shfl_xor(sum, 1); sum += __shfl_xor(sum, 2); sum += __shfl_xor(sum, 4);
    const float mean = sum * (1.0f / 576.0f);
    float vs = 0.f;
#pragma unroll
    for (int i = 0; i < 9; ++i)
#pragma unroll
      for (int j = 0; j < 4; ++j) {
        float a = lo_bf(raw[i][j]) - mean, b = hi_bf(raw[i][j]) - mean;
        vs += a * a + b * b;
      }
    vs += __shfl_xor(vs, 1); vs += __shfl_xor(vs, 2); vs += __shfl_xor(vs, 4);
    const float mb = rbf(mean);
    const float vb = rbf(vs * (1.0f / 576.0f));
    const float sd = rbf(sqrtf(rbf(vb + 1e-5f)));
    const float rs = 1.0f / sd;
#pragma unroll
    for (int i = 0; i < 9; ++i) {
      u32x4 o;
#pragma unroll
      for (int j = 0; j < 4; ++j) {
        float a = rbf(lo_bf(raw[i][j]) - mb), b = rbf(hi_bf(raw[i][j]) - mb);
        float qa = a * rs, qb = b * rs;
        qa = fmaf(fmaf(-qa, sd, a), rs, qa); qb = fmaf(fmaf(-qb, sd, b), rs, qb);
        o[j] = pack2(qa, qb);
      }
      *(u32x4*)(smem + L_XN + tok * 1168 + (part + 8 * i) * 16) = o;
    }
    bar();
  }

  if (wid < 4) {
    const int pmt = wid >> 1, pj = wid & 1;
    bf16x8 xn[36];
#pragma unroll
    for (int s = 0; s < 36; ++s)
      xn[s] = *(const bf16x8*)(smem + L_XN + (pmt * 32 + r) * 1168 + (16 * s + 8 * h) * 2);
    {
      f32x16 qa[4];
#pragma unroll
      for (int t = 0; t < 4; ++t)
#pragma unroll
        for (int i = 0; i < 16; ++i) qa[t][i] = 0.f;
      const int lb = (8 * h + q) * 448 + (pj * 4) * 64 + blk * 32 + p * 8;
#pragma unroll
      for (int kc = 0; kc < 9; ++kc) {
        bar();
        const char* bb = smem + L_QST + (kc % 3) * 28672 + lb;
#pragma unroll
        for (int ks = 0; ks < 4; ++ks) {
#pragma unroll
          for (int t = 0; t < 4; ++t) {
            s16x4 a0 = tr_read(bb, ks * 16 * 448 + t * 64);
            s16x4 a1 = tr_read(bb, ks * 16 * 448 + 4 * 448 + t * 64);
            qa[t] = MFMA32(cat8(a0, a1), xn[kc * 4 + ks], qa[t]);
          }
        }
      }
      char* qt = smem + L_QKV + (pmt * 32 + r) * 464;
#pragma unroll
      for (int t = 0; t < 4; ++t) {
        if (t < 3 || pj == 0) {
#pragma unroll
          for (int g = 0; g < 4; ++g) {
            u32x2 v = {pack2(qa[t][4 * g], qa[t][4 * g + 1]), pack2(qa[t][4 * g + 2], qa[t][4 * g + 3])};
            *(u32x2*)(qt + ((pj * 4 + t) * 32 + 8 * g + 4 * h) * 2) = v;
          }
        }
      }
      bar();
    }
    int loff[9];
#pragma unroll
    for (int e = 0; e < 9; ++e)
      loff[e] = ((lhf * 9 + e) * 8 + (lane >> 3)) * F_ + (((lane & 7) ^ (((lane >> 4) & 1) << 2)) * 8);
    auto issue_w1 = [&](int tau) {
      const u16* g = w1 + (size_t)(tau & 3) * 144 * F_ + c * FC + (tau >> 2) * 64;
      char* l = smem + L_W1 + (tau & 3) * 18432 + lhf * 9216;
#pragma unroll
      for (int e = 0; e < 9; ++e) glds16(g + loff[e], l + e * 1024);
    };
    if (lpar == 1) { issue_w1(0); issue_w1(2); } else { issue_w1(1); }
    waitv0();
    bar();
    const int lb = (8 * h + q) * 128 + 16 * ((4 * pj + 2 * blk + (p >> 1)) ^ (((q >> 1) & 1) << 2)) + 8 * (p & 1);
    char* xdst = smem + L_X + (pmt * 2 + pj) * 2048 + lane * 16;
    f32x16 xa, xp;
#pragma unroll
    for (int i = 0; i < 16; ++i) { xa[i] = 0.f; xp[i] = 0.f; }
    for (int it = 0; it < 11; ++it) {
#pragma unroll
      for (int qd = 0; qd < 4; ++qd) {
        const int tau = it * 4 + qd;
        if ((qd & 1) == lpar && tau + 3 < 36) issue_w1(tau + 3);
        if (it < 9) {
          const char* bb = smem + L_W1 + qd * 18432 + lb;
#pragma unroll
          for (int ks = 0; ks < 9; ++ks) {
            s16x4 a0 = tr_read(bb, ks * 2048);
            s16x4 a1 = tr_read(bb, ks * 2048 + 512);
            xa = MFMA32(cat8(a0, a1), xn[qd * 9 + ks], xa);
          }
        }
        if ((qd == 1 || qd == 2) && it >= 1 && it <= 9) {
          const int s = qd - 1;
          const float* bp = (const float*)(smem + L_B1) + (it - 1) * 64 + pj * 32 + 16 * s + 4 * h;
          f32x4 bA = *(const f32x4*)(bp), bB = *(const f32x4*)(bp + 8);
          float g[8];
#pragma unroll
          for (int j = 0; j < 4; ++j) {
            g[j] = gelu_tanh(rbf(rbf(xp[8 * s + j]) + bA[j]));
            g[4 + j] = gelu_tanh(rbf(rbf(xp[8 * s + 4 + j]) + bB[j]));
          }
          u32x4 v = {pack2(g[0], g[1]), pack2(g[2], g[3]), pack2(g[4], g[5]), pack2(g[6], g[7])};
          *(u32x4*)(xdst + s * 1024) = v;
        }
        if (qd == 3) {
          xp = xa;
#pragma unroll
          for (int i = 0; i < 16; ++i) xa[i] = 0.f;
        }
        if (tau + 3 < 36) { WAITV(9); } else { WAITV(0); }
        bar();
      }
    }
    if (blockIdx.x < 72) {
      const int k0 = blockIdx.x * 8;
      for (int n = tid; n < 576; n += 256) {
        unsigned o[4];
#pragma unroll
        for (int j = 0; j < 4; ++j)
          o[j] = (unsigned)wo[(size_t)(k0 + 2 * j) * D_ + n] | ((unsigned)wo[(size_t)(k0 + 2 * j + 1) * D_ + n] << 16);
        u32x4 v = {o[0], o[1], o[2], o[3]};
        *(u32x4*)(ws + WS_WOT + ((size_t)n * D_ + k0) * 2) = v;
      }
    }
    if (wid == 0) {
      unsigned* cnt = (unsigned*)(ws + WS_CNT) + c * 16;
      for (int spin = 0; spin < (1 << 22); ++spin) {
        unsigned v = __hip_atomic_load(cnt, __ATOMIC_RELAXED, __HIP_MEMORY_SCOPE_AGENT);
        if (v >= (unsigned)NMT) break;
        __builtin_amdgcn_s_sleep(2);
      }
      for (int e = lane; e < 90; e += 64) {
        const int idx = c * 4 * 720 + mt64 * 90 + e;
        const float* src = (const float*)(ws + WS_KVP) + idx;
        float s = 0.f;
#pragma unroll 8
        for (int m = 0; m < NMT; ++m)
          s += __hip_atomic_load(src + (size_t)m * 32 * 720, __ATOMIC_RELAXED, __HIP_MEMORY_SCOPE_AGENT);
        ((float*)(ws + WS_KVR))[idx] = s;
      }
    }
  } else {
    const int cw = wid - 4;
    {
      for (int kc = 0; kc < 9; ++kc) {
        if (kc <= 7) { WAITV(7); } else { WAITV(0); }
        bar();
        if (kc + 2 < 9) issue_qkv(kc + 2);
      }
      bar();
      const int ct = tid - 256;
      for (int i = ct; i < 576; i += 256) ((float*)(smem + L_B1))[i] = bf2f(b1[c * FC + i]);
      for (int i = ct; i < 4 * 18 * 18; i += 256) {
        int hd = i / 324, rem = i % 324, d = rem / 18, f = rem % 18;
        ((float*)(smem + L_WPHI))[(hd * 18 + d) * 20 + f] = bf2f(wphi[(c * 4) * 324 + i]);
      }
      bar();
    }
    int loff[9];
#pragma unroll
    for (int e = 0; e < 9; ++e) {
      int pp = (lhf * 9 + e) * 64 + lane, row = pp / 72, ch = pp % 72;
      loff[e] = row * D_ + ((ch ^ (((row >> 1) & 1) << 2)) * 8);
    }
    auto issue_w2 = [&](int tau) {
      const u16* g = w2 + (size_t)(c * FC + (tau - 8) * 16) * D_;
      char* l = smem + L_W2 + (tau & 3) * 18432 + lhf * 9216;
#pragma unroll
      for (int e = 0; e < 9; ++e) glds16(g + loff[e], l + e * 1024);
    };
    const int hd = cw;
#pragma unroll 1
    for (int qk = 0; qk < 2; ++qk) {
      const unsigned* qsrc = (const unsigned*)(smem + L_QKV + lane * 464 + (qk * 72 + hd * 18) * 2);
      float qv[18];
#pragma unroll
      for (int i = 0; i < 9; ++i) { unsigned u = qsrc[i]; qv[2 * i] = lo_bf(u); qv[2 * i + 1] = hi_bf(u); }
      const float* wp = (const float*)(smem + L_WPHI) + hd * 18 * 20;
      float pq[18];
#pragma unroll
      for (int f = 0; f < 18; ++f) pq[f] = 0.f;
#pragma unroll
      for (int d = 0; d < 18; ++d) {
#pragma unroll
        for (int f = 0; f < 18; ++f) pq[f] = fmaf(qv[d], wp[d * 20 + f], pq[f]);
      }
      float mx = -3.0e38f, mn = 3.0e38f;
#pragma unroll
      for (int f = 0; f < 18; ++f) { pq[f] = rbf(pq[f]); mx = fmaxf(mx, pq[f]); mn = fminf(mn, pq[f]); }
      float ep[18], en[18], sp = 0.f, sn = 0.f;
#pragma unroll
      for (int f = 0; f < 18; ++f) {
        ep[f] = __expf(pq[f] - mx); sp += ep[f];
        en[f] = __expf(mn - pq[f]); sn += en[f];
      }
      const float rp = 1.0f / sp, rn = 1.0f / sn;
      if (qk == 0) {
        unsigned o[20];
#pragma unroll
        for (int i = 0; i < 9; ++i) {
          o[i] = pack2(ep[2 * i] * rp, ep[2 * i + 1] * rp);
          o[9 + i] = pack2(en[2 * i] * rn, en[2 * i + 1] * rn);
        }
        o[18] = 0; o[19] = 0;
        u32x4* dst = (u32x4*)(ws + WS_PHIQ + ((size_t)(m0 + lane) * 32 + c * 4 + hd) * 80);
#pragma unroll
        for (int i = 0; i < 5; ++i) { u32x4 v = {o[4 * i], o[4 * i + 1], o[4 * i + 2], o[4 * i + 3]}; dst[i] = v; }
      } else {
        u16* pk = (u16*)(smem + L_PHIK + hd * 36 * 128) + lane;
#pragma unroll
        for (int f = 0; f < 18; ++f) { pk[f * 64] = f2bf(ep[f] * rp); pk[(18 + f) * 64] = f2bf(en[f] * rn); }
        const unsigned* vsrc = (const unsigned*)(smem + L_QKV + lane * 464 + (144 + hd * 18) * 2);
        u16* vt = (u16*)(smem + L_VT + hd * 19 * 128) + lane;
#pragma unroll
        for (int i = 0; i < 9; ++i) { unsigned u = vsrc[i]; vt[(2 * i) * 64] = (u16)(u & 0xffff); vt[(2 * i + 1) * 64] = (u16)(u >> 16); }
        vt[18 * 64] = 0x3f80;
      }
      bar();
    }
    {
      f32x16 kd[2];
#pragma unroll
      for (int t = 0; t < 2; ++t)
#pragma unroll
        for (int i = 0; i < 16; ++i) kd[t][i] = 0.f;
#pragma unroll
      for (int s = 0; s < 4; ++s) {
        bf16x8 bv = *(const bf16x8*)(smem + L_VT + (hd * 19 + r) * 128 + (16 * s + 8 * h) * 2);
#pragma unroll
        for (int t = 0; t < 2; ++t) {
          bf16x8 av = *(const bf16x8*)(smem + L_PHIK + (hd * 36 + t * 32 + r) * 128 + (16 * s + 8 * h) * 2);
          kd[t] = MFMA32(av, bv, kd[t]);
        }
      }
      float* kp = (float*)(ws + WS_KVP) + ((size_t)mt64 * 32 + c * 4 + hd) * 720;
      if (r < 19) {
#pragma unroll
        for (int t = 0; t < 2; ++t)
#pragma unroll
          for (int i = 0; i < 16; ++i) {
            int f = t * 32 + (i & 3) + 8 * (i >> 2) + 4 * h;
            if (f < 36) __hip_atomic_store(kp + f * 20 + r, kd[t][i], __ATOMIC_RELAXED, __HIP_MEMORY_SCOPE_AGENT);
          }
      }
      bar();
    }
    bar();
    waitv0();
    bar();
    if (tid == 256)
      __hip_atomic_fetch_add((unsigned*)(ws + WS_CNT) + c * 16, 1u, __ATOMIC_RELAXED, __HIP_MEMORY_SCOPE_AGENT);
    waitv0();
#pragma unroll
    for (int tau = 5; tau < 8; ++tau) {
      if ((tau & 1) == lpar) issue_w2(tau + 3);
      WAITV(9);
      bar();
    }
    const int cmt = cw >> 1, nh = cw & 1;
    const int sw = (q >> 1) & 1;
    const int rowoff = (4 * h + q) * 1152 + nh * 576 + 16 * (2 * blk + (p >> 1)) + 8 * (p & 1);
    const int baseA = rowoff + (nh ? -64 * sw : 64 * sw);
    const int baseB = rowoff + (nh ? 64 * sw : -64 * sw);
    f32x16 acc[9];
#pragma unroll
    for (int n = 0; n < 9; ++n)
#pragma unroll
      for (int i = 0; i < 16; ++i) acc[n][i] = 0.f;
    bf16x8 xf[4];
    for (int it = 2; it < 11; ++it) {
#pragma unroll
      for (int qd = 0; qd < 4; ++qd) {
        const int tau = it * 4 + qd;
        if ((qd & 1) == lpar && tau + 3 < 44) issue_w2(tau + 3);
        if (qd == 0) {
#pragma unroll
          for (int u = 0; u < 4; ++u)
            xf[u] = *(const bf16x8*)(smem + L_X + (cmt * 2 + (u >> 1)) * 2048 + (u & 1) * 1024 + lane * 16);
        }
        const char* bb = smem + L_W2 + qd * 18432;
#pragma unroll
        for (int n = 0; n < 9; ++n) {
          const int base = (n & 1) ? baseB : baseA;
          s16x4 b0 = tr_read(bb + base, n * 64);
          s16x4 b1v = tr_read(bb + base, n * 64 + 8 * 1152);
          acc[n] = MFMA32(xf[qd], cat8(b0, b1v), acc[n]);
        }
        if (tau + 3 < 44) { WAITV(9); } else { WAITV(0); }
        bar();
      }
    }
    float* pp = (float*)(ws + WS_PART) + ((size_t)c * S_ + m0 + cmt * 32 + 4 * h) * D_ + nh * 288 + r;
#pragma unroll
    for (int n = 0; n < 9; ++n)
#pragma unroll
      for (int i = 0; i < 16; ++i) pp[(size_t)((i & 3) + 8 * (i >> 2)) * D_ + n * 32] = acc[n][i];
  }
}

constexpr int L2_KVT = 0;
constexpr int L2_O   = 61440 + 4096;
constexpr int LDS2   = L2_O + 32 * 1168;

__global__ __launch_bounds__(256)
void k2(const u16* __restrict__ x, const u16* __restrict__ b2, const char* __restrict__ ws, u16* __restrict__ out) {
  extern __shared__ __attribute__((aligned(16))) char smem[];
  const int tid = threadIdx.x, lane = tid & 63;
  const int wid = __builtin_amdgcn_readfirstlane(tid >> 6);
  const int rt = blockIdx.x >> 2, ct = blockIdx.x & 3;
  const int r = lane & 31, h = lane >> 5;

  const float* kvr = (const float*)(ws + WS_KVR);
  for (int i = tid; i < 32 * 20 * 24; i += 256) {
    int hd = i / 480, rem = i % 480, d = rem / 24, f2 = (rem % 24) * 2;
    float a = 0.f, b = 0.f;
    if (f2 < 36) { a = kvr[hd * 720 + f2 * 20 + d]; b = kvr[hd * 720 + (f2 + 1) * 20 + d]; }
    ((unsigned*)(smem + L2_KVT))[i] = pack2(a, b);
  }
  for (int i = tid; i < 1024; i += 256) ((unsigned*)(smem + L2_KVT + 61440))[i] = 0u;
  __syncthreads();

  {
    const char* pq = ws + WS_PHIQ + ((size_t)(rt * 32 + r) * 32) * 80 + 16 * h;
#pragma unroll 2
    for (int hh = 0; hh < 8; ++hh) {
      const int hd = wid * 8 + hh;
      f32x16 d16;
#pragma unroll
      for (int i = 0; i < 16; ++i) d16[i] = 0.f;
#pragma unroll
      for (int s = 0; s < 3; ++s) {
        u32x4 av = *(const u32x4*)(pq + hd * 80 + s * 32);
        if (s == 2 && h == 1) { av[0] = 0; av[1] = 0; av[2] = 0; av[3] = 0; }
        bf16x8 bv = *(const bf16x8*)(smem + L2_KVT + (hd * 20 + r) * 96 + (16 * s + 8 * h) * 2);
        d16 = MFMA32(__builtin_bit_cast(bf16x8, av), bv, d16);
      }
#pragma unroll
      for (int i = 0; i < 16; ++i) {
        float den = __shfl(d16[i], 18 + 32 * h);
        den = rbf(rbf(den) + 1e-6f);
        float o = rbf(d16[i]) / den;
        if (r < 18) {
          int row = (i & 3) + 8 * (i >> 2) + 4 * h;
          *(u16*)(smem + L2_O + row * 1168 + (hd * 18 + r) * 2) = f2bf(o);
        }
      }
    }
  }
  __syncthreads();

  const int mt = wid & 1, nb = (wid >> 1) * 5, nn = (wid >> 1) ? 4 : 5;
  const int fr = lane & 15, fq = lane >> 4;
  f32x4 sa[5];
#pragma unroll
  for (int n = 0; n < 5; ++n) sa[n] = (f32x4){0.f, 0.f, 0.f, 0.f};
  const u16* wot = (const u16*)(ws + WS_WOT);
#pragma unroll 3
  for (int ks = 0; ks < 18; ++ks) {
    bf16x8 av = *(const bf16x8*)(smem + L2_O + (mt * 16 + fr) * 1168 + (32 * ks + 8 * fq) * 2);
#pragma unroll
    for (int n = 0; n < 5; ++n) {
      if (n < nn) {
        bf16x8 bv = *(const bf16x8*)(wot + (size_t)(ct * 144 + (nb + n) * 16 + fr) * D_ + 32 * ks + 8 * fq);
        sa[n] = MFMA16(av, bv, sa[n]);
      }
    }
  }
  const float* part = (const float*)(ws + WS_PART);
#pragma unroll
  for (int n = 0; n < 5; ++n) {
    if (n < nn) {
      const int col = ct * 144 + (nb + n) * 16 + fr;
      const float bb = bf2f(b2[col]);
#pragma unroll
      for (int j = 0; j < 4; ++j) {
        const int row = rt * 32 + mt * 16 + fq * 4 + j;
        float f = 0.f;
#pragma unroll
        for (int cc = 0; cc < NCH; ++cc) f += part[((size_t)cc * S_ + row) * D_ + col];
        f = rbf(rbf(f) + bb);
        float g = rbf(f / (1.0f + __expf(-f)));
        float sg = rbf(rbf(sa[n][j]) * g);
        out[(size_t)row * D_ + col] = f2bf(bf2f(x[(size_t)row * D_ + col]) + sg);
      }
    }
  }
}

extern "C" void kernel_launch(void* const* d_in, const int* in_sizes, int n_in,
                              void* d_out, int out_size, void* d_ws, size_t ws_size,
                              hipStream_t stream) {
  const u16* x = (const u16*)d_in[0];
  const u16* wqkv = (const u16*)d_in[1];
  const u16* wphi = (const u16*)d_in[2];
  const u16* wo = (const u16*)d_in[3];
  const u16* w1 = (const u16*)d_in[5];
  const u16* b1 = (const u16*)d_in[6];
  const u16* w2 = (const u16*)d_in[7];
  const u16* b2 = (const u16*)d_in[8];
  char* ws = (char*)d_ws;
  hipFuncSetAttribute((const void*)k1, hipFuncAttributeMaxDynamicSharedMemorySize, LDS1);
  hipFuncSetAttribute((const void*)k2, hipFuncAttributeMaxDynamicSharedMemorySize, LDS2);
  hipMemsetAsync(ws + WS_CNT, 0, 1024, stream);
  k1<<<256, 512, LDS1, stream>>>(x, wqkv, wphi, wo, w1, b1, w2, ws);
  k2<<<256, 256, LDS2, stream>>>(x, b2, ws, (u16*)d_out);
}
```

```cpp
#include <hip/hip_runtime.h>
#include <hip/hip_bf16.h>
#include <stdint.h>
#include <type_traits>

typedef __attribute__((ext_vector_type(8))) __bf16 bf16x8;
typedef __attribute__((ext_vector_type(2))) __bf16 bf16x2;
typedef __attribute__((ext_vector_type(4))) short s16x4;
typedef __attribute__((ext_vector_type(8))) short s16x8;
typedef __attribute__((ext_vector_type(16))) float f32x16;
typedef __attribute__((ext_vector_type(4))) float f32x4;
typedef __attribute__((ext_vector_type(2))) float f32x2;
typedef __attribute__((ext_vector_type(4))) unsigned u32x4;
typedef __attribute__((ext_vector_type(2))) unsigned u32x2;
typedef unsigned short u16;
typedef __attribute__((address_space(3))) s16x4* lds_s16x4_p;

#define DI __device__ __forceinline__

constexpr int S_ = 2048, D_ = 576, F_ = 4608;
constexpr int NCH = 8;
constexpr int FC = F_ / NCH;
constexpr int NMT = S_ / 64;

constexpr size_t WS_CNT  = 0;
constexpr size_t WS_KVP  = 1024;
constexpr size_t WS_KVT  = WS_KVP + (size_t)NMT * 32 * 800 * 4;
constexpr size_t WS_PHIQ = WS_KVT + (size_t)32 * 960 * 2 + 4096;
constexpr size_t WS_WOT  = WS_PHIQ + (size_t)S_ * 32 * 40 * 2 + 256;
constexpr size_t WS_PART = WS_WOT + (size_t)D_ * D_ * 2;

constexpr int L_W1   = 0;
constexpr int L_W2   = 73728;
constexpr int L_QST  = 0;
constexpr int L_XN   = 86016;
constexpr int L_QT   = 73728;
constexpr int L_PK   = L_QT + 64 * 784;
constexpr int L_X    = 147456;
constexpr int L_B1   = 155648;
constexpr int LDS1   = 163840;

DI float bf2f(u16 v) { return __uint_as_float(((unsigned)v) << 16); }
DI float rbf(float f) { return (float)((__bf16)f); }
DI u16 f2bf(float f) { __bf16 b = (__bf16)f; return __builtin_bit_cast(u16, b); }
DI unsigned pack2(float a, float b) {
  f32x2 v = {a, b};
  bf16x2 p = __builtin_convertvector(v, bf16x2);
  return __builtin_bit_cast(unsigned, p);
}
DI float lo_bf(unsigned u) { return __uint_as_float(u << 16); }
DI float hi_bf(unsigned u) { return __uint_as_float(u & 0xffff0000u); }
DI float fdiv(float a, float b) {
  float r = __builtin_amdgcn_rcpf(b);
  float q = a * r;
  return fmaf(fmaf(-q, b, a), r, q);
}

DI void waitv0()  { asm volatile("s_waitcnt vmcnt(0)" ::: "memory"); }
DI void bar() {
  asm volatile("s_waitcnt lgkmcnt(0)" ::: "memory");
  __builtin_amdgcn_s_barrier();
  asm volatile("" ::: "memory");
}
#define TBAR() bar()
#define WAITV(n) asm volatile("s_waitcnt vmcnt(" #n ")" ::: "memory")

DI s16x4 tr_read(const char* base, int off) {
  return __builtin_amdgcn_ds_read_tr16_b64_v4i16((lds_s16x4_p)(base + off));
}
DI bf16x8 cat8(s16x4 a, s16x4 b) {
  s16x8 v = __builtin_shufflevector(a, b, 0, 1, 2, 3, 4, 5, 6, 7);
  return __builtin_bit_cast(bf16x8, v);
}
DI void glds16(const void* base, unsigned voff, unsigned lds) {
  unsigned keep;
  asm volatile("s_mov_b32 %0, m0\n\ts_mov_b32 m0, %3\n\ts_nop 0\n\tglobal_load_lds_dwordx4 %1, %2\n\ts_mov_b32 m0, %0"
               : "=&s"(keep) : "v"(voff), "s"(base), "s"(lds) : "memory");
}
#define MFMA32(a, b, c) __builtin_amdgcn_mfma_f32_32x32x16_bf16((a), (b), (c), 0, 0, 0)
#define MFMA16(a, b, c) __builtin_amdgcn_mfma_f32_16x16x32_bf16((a), (b), (c), 0, 0, 0)

#define STAMP(i)
#define STAMP2(i)
#define CSTAMP(i)

DI float gelu_tanh(float x) {
  float t = fmaf(x * x, -0.10294324f, -2.30220820f);
  float e = __builtin_amdgcn_exp2f(x * t);
  return x * __builtin_amdgcn_rcpf(1.0f + e);
}

__global__ __launch_bounds__(512, 2)
void k1(const u16* __restrict__ x, const u16* __restrict__ wqkv, const u16* __restrict__ wphi,
        const u16* __restrict__ wo, const u16* __restrict__ w1, const u16* __restrict__ b1,
        const u16* __restrict__ w2, char* __restrict__ ws) {
  extern __shared__ __attribute__((aligned(16))) char smem[];
  const unsigned lds0 = (unsigned)(size_t)smem;
  const int tid = threadIdx.x, lane = tid & 63;
  const int wid = __builtin_amdgcn_readfirstlane(tid >> 6);
  const int c = blockIdx.x & 7, mt64 = blockIdx.x >> 3, m0 = mt64 * 64;
  const int r = lane & 31, h = lane >> 5;
  const int blk = (lane >> 4) & 1, q = (lane & 15) >> 2, p = lane & 3;
  const int lpar = (wid >> 1) & 1, lhf = wid & 1;

  STAMP(0);
  unsigned goff[7];
  if (wid >= 4) {
#pragma unroll
    for (int e = 0; e < 7; ++e) {
      int pp = ((wid - 4) * 7 + e) * 64 + lane, row = pp / 28, ch = pp % 28;
      if (ch == 27) ch = 26;
      goff[e] = (unsigned)(row * 1728 + (ch / 9) * 576 + c * 72 + (ch % 9) * 8) * 2u;
    }
  }
  auto issue_qkv = [&](int kc) {
    const u16* g = wqkv + (size_t)kc * 64 * 1728;
    const unsigned l = lds0 + L_QST + (kc % 5) * 28672 + (wid - 4) * 7 * 1024;
#pragma unroll
    for (int e = 0; e < 7; ++e) glds16(g, goff[e], l + e * 1024);
  };

  {
    const int tok = tid >> 3, part = tid & 7;
    const u16* xr = x + (size_t)(m0 + tok) * D_ + part * 8;
    u32x4 raw[9];
#pragma unroll
    for (int i = 0; i < 9; ++i) raw[i] = *(const u32x4*)(xr + 64 * i);
    u32x4 pfv[6];
    {
      const int i0 = tid;
      const int rw = i0 / 27, ch = i0 - rw * 27;
      const u16* a = wqkv + (size_t)(mt64 * 18 + (i0 < 486 ? rw : 0)) * 1728 + (ch / 9) * 576 + c * 72 + (ch % 9) * 8;
      asm volatile("global_load_dwordx4 %0, %1, off" : "=v"(pfv[0]) : "v"(a));
#pragma unroll
      for (int k = 0; k < 3; ++k) {
        const int i1 = tid + 512 * k, rw1 = i1 / 72, ch1 = i1 - rw1 * 72;
        const u16* b = w1 + (size_t)(mt64 * 18 + (i1 < 1296 ? rw1 : 0)) * F_ + c * FC + ch1 * 8;
        if (k < 2) asm volatile("global_load_dwordx4 %0, %1, off" : "=v"(pfv[1 + k]) : "v"(b));
        else {
          const u16* b2p = w2 + (size_t)(c * FC + mt64 * 18) * D_ + (size_t)(1024 + (tid - 272)) * 8;
          const u16* sel = (tid < 272) ? b : b2p;
          asm volatile("global_load_dwordx4 %0, %1, off" : "=v"(pfv[3]) : "v"(sel));
        }
      }
#pragma unroll
      for (int k = 0; k < 2; ++k) {
        const u16* d = w2 + (size_t)(c * FC + mt64 * 18) * D_ + (size_t)(tid + 512 * k) * 8;
        asm volatile("global_load_dwordx4 %0, %1, off" : "=v"(pfv[4 + k]) : "v"(d));
      }
    }
    float sum = 0.f;
#pragma unroll
    for (int i = 0; i < 9; ++i)
#pragma unroll
      for (int j = 0; j < 4; ++j) sum += lo_bf(raw[i][j]) + hi_bf(raw[i][j]);
    STAMP(7);
    if (wid >= 4) { issue_qkv(0); issue_qkv(1); issue_qkv(2); }
    STAMP(8);
    sum += __shfl_xor(sum, 1); sum += __shfl_xor(sum, 2); sum += __shfl_xor(sum, 4);
    const float mean = sum * (1.0f / 576.0f);
    float vs = 0.f;
#pragma unroll
    for (int i = 0; i < 9; ++i)
#pragma unroll
      for (int j = 0; j < 4; ++j) {
        float a = lo_bf(raw[i][j]) - mean, b = hi_bf(raw[i][j]) - mean;
        vs += a * a + b * b;
      }
    vs += __shfl_xor(vs, 1); vs += __shfl_xor(vs, 2); vs += __shfl_xor(vs, 4);
    const float mb = rbf(mean);
    const float vb = rbf(vs * (1.0f / 576.0f));
    const float sd = rbf(sqrtf(rbf(vb + 1e-5f)));
    const float rs = __builtin_amdgcn_rcpf(sd);
    STAMP(9);
#pragma unroll
    for (int i = 0; i < 9; ++i) {
      u32x4 o;
#pragma unroll
      for (int j = 0; j < 4; ++j) {
        float a = rbf(lo_bf(raw[i][j]) - mb), b = rbf(hi_bf(raw[i][j]) - mb);
        float qa = a * rs, qb = b * rs;
        qa = fmaf(fmaf(-qa, sd, a), rs, qa); qb = fmaf(fmaf(-qb, sd, b), rs, qb);
        o[j] = pack2(qa, qb);
      }
      *(u32x4*)(smem + L_XN + tok * 1168 + (part + 8 * i) * 16) = o;
    }
    STAMP(10);
    if (wid < 4) { WAITV(0); } else { WAITV(21); }
    asm volatile("" :: "v"(pfv[0]), "v"(pfv[1]), "v"(pfv[2]), "v"(pfv[3]), "v"(pfv[4]), "v"(pfv[5]));
    bar();
  }
  STAMP(1);

  if (wid < 4) {
    const int pmt = wid >> 1, pj = wid & 1;
    bf16x8 xn[36];
#pragma unroll
    for (int s = 0; s < 36; ++s)
      xn[s] = *(const bf16x8*)(smem + L_XN + (pmt * 32 + r) * 1168 + (16 * s + 8 * h) * 2);
    bar();
    STAMP(11);
    {
      f32x16 qa[4];
#pragma unroll
      for (int t = 0; t < 4; ++t)
#pragma unroll
        for (int i = 0; i < 16; ++i) qa[t][i] = 0.f;
      const int lb = (8 * h + q) * 448 + (pj * 4) * 64 + blk * 32 + p * 8;
#pragma unroll
      for (int kc = 0; kc < 9; ++kc) {
        bar();
        int lbk = lb + L_QST + (kc % 5) * 28672; asm volatile("" : "+v"(lbk));
        const char* bb = smem + lbk;
#pragma unroll
        for (int ks = 0; ks < 4; ++ks) {
#pragma unroll
          for (int t = 0; t < 4; ++t) {
            s16x4 a0 = tr_read(bb, ks * 16 * 448 + t * 64);
            s16x4 a1 = tr_read(bb, ks * 16 * 448 + 4 * 448 + t * 64);
            qa[t] = MFMA32(cat8(a0, a1), xn[kc * 4 + ks], qa[t]);
          }
        }
      }
      STAMP(12);
      bar();
      int lz = lane; asm volatile("" : "+v"(lz));
      const int h = lz >> 5;
      char* qt = smem + L_QT + (pmt * 32 + (lz & 31)) * 784;
#pragma unroll
      for (int t = 0; t < 4; ++t) {
#pragma unroll
        for (int g = 0; g < 4; ++g) {
#pragma unroll
          for (int e = 0; e < 2; ++e) {
            const int u = (pj * 4 + t) * 32 + 8 * g + 4 * h + 2 * e;
            const int sec = u / 72, rem = u - sec * 72, hd = rem / 18, d = rem - hd * 18;
            if (u < 216) *(unsigned*)(qt + (sec * 128 + hd * 32 + d) * 2) = pack2(qa[t][4 * g + 2 * e], qa[t][4 * g + 2 * e + 1]);
          }
        }
      }
    }
    STAMP(2);
    const int e0 = (wid & 3) < 2 ? (wid & 3) * 5 : 10 + ((wid & 3) - 2) * 4;
    const bool five = (wid & 3) < 2;
    unsigned loff[5];
#pragma unroll
    for (int e = 0; e < 5; ++e)
      loff[e] = (unsigned)(((e0 + e) * 8 + (lane >> 3)) * F_ + (((lane & 7) ^ (((lane >> 4) & 1) << 2)) * 8)) * 2u;
    auto issue_w1 = [&](int tau, int e) {
      const u16* g = w1 + (size_t)(tau & 3) * 144 * F_ + c * FC + (tau >> 2) * 64;
      glds16(g, loff[e], lds0 + L_W1 + (tau & 3) * 18432 + (e0 + e) * 1024);
    };
#pragma unroll
    for (int t = 0; t < 3; ++t) {
#pragma unroll
      for (int e = 0; e < 4; ++e) issue_w1(t, e);
      if (five) issue_w1(t, 4);
    }
    if (five) { WAITV(5); } else { WAITV(4); }
    bar();
    STAMP(3);
    const int lb = (8 * h + q) * 128 + 16 * ((4 * pj + 2 * blk + (p >> 1)) ^ (((q >> 1) & 1) << 2)) + 8 * (p & 1);
    const char* wb = smem + L_W1 + lb;
    char* xdst = smem + L_X + (pmt * 2 + pj) * 2048 + lane * 16;
    f32x16 xa, xp;
#pragma unroll
    for (int i = 0; i < 16; ++i) { xa[i] = 0.f; xp[i] = 0.f; }
    bf16x8 fr[6];
#pragma unroll
    for (int ks = 0; ks < 6; ++ks) fr[ks] = cat8(tr_read(wb, ks * 2048), tr_read(wb, ks * 2048 + 512));
    auto iteration = [&](auto DOM, auto DOG, int it, f32x16& xc, const f32x16& xq) {
      constexpr bool doM = decltype(DOM)::value, doG = decltype(DOG)::value;
#pragma unroll
      for (int qd = 0; qd < 4; ++qd) {
        const int tau = it * 4 + qd;
        const bool ld = tau + 3 < 36;
        CSTAMP(qd * 3);
        if (doM && qd == 0) {
          const float* bp = (const float*)(smem + L_B1) + it * 64 + pj * 32 + 4 * h;
#pragma unroll
          for (int g4 = 0; g4 < 4; ++g4) {
            const f32x4 bv = *(const f32x4*)(bp + 8 * g4);
#pragma unroll
            for (int j = 0; j < 4; ++j) xc[4 * g4 + j] = bv[j];
          }
        }
        if (doG && qd == 0) {
          u32x4 v0 = {pack2(xq[0], xq[1]), pack2(xq[2], xq[3]), pack2(xq[4], xq[5]), pack2(xq[6], xq[7])};
          u32x4 v1 = {pack2(xq[8], xq[9]), pack2(xq[10], xq[11]), pack2(xq[12], xq[13]), pack2(xq[14], xq[15])};
          *(u32x4*)(xdst) = v0; *(u32x4*)(xdst + 1024) = v1;
        }
#pragma unroll
        for (int ks = 0; ks < 9; ++ks) {
          if (doM) {
            const int g = qd * 9 + ks, gn = (g + 6) % 36;
            const int noff = (gn / 9) * 18432 + (gn % 9) * 2048;
            xc = MFMA32(fr[g % 6], xn[g], xc);
            fr[g % 6] = cat8(tr_read(wb, noff), tr_read(wb, noff + 512));
            if (ld && ks < 4) issue_w1(tau + 3, ks);
          }
        }
        if (doM && ld && five) issue_w1(tau + 3, 4);
        CSTAMP(qd * 3 + 1);
        CSTAMP(qd * 3 + 2);
        if (!(doM && ld)) { WAITV(0); } else if (five) { WAITV(5); } else { WAITV(4); }
        TBAR();
      }
    };
    using T_ = std::integral_constant<bool, true>; using F_t = std::integral_constant<bool, false>;
    iteration(T_{}, F_t{}, 0, xa, xp);
#pragma unroll 1
    for (int it = 1; it < 9; it += 2) {
      iteration(T_{}, T_{}, it, xp, xa);
      if (it == 1) STAMP(4);
      iteration(T_{}, T_{}, it + 1, xa, xp);
    }
    iteration(F_t{}, T_{}, 9, xp, xa);
    iteration(F_t{}, F_t{}, 10, xp, xa);
    STAMP(5);
    if (blockIdx.x < 72) {
      const int k0 = blockIdx.x * 8;
      for (int n = tid; n < 576; n += 256) {
        unsigned o[4];
#pragma unroll
        for (int j = 0; j < 4; ++j)
          o[j] = (unsigned)wo[(size_t)(k0 + 2 * j) * D_ + n] | ((unsigned)wo[(size_t)(k0 + 2 * j + 1) * D_ + n] << 16);
        u32x4 v = {o[0], o[1], o[2], o[3]};
        *(u32x4*)(ws + WS_WOT + ((size_t)n * D_ + k0) * 2) = v;
      }
    }
    if (wid < 2) {
      unsigned* cnt = (unsigned*)(ws + WS_CNT) + c * 16;
      for (int spin = 0; spin < (1 << 22); ++spin) {
        unsigned v = __hip_atomic_load(cnt, __ATOMIC_RELAXED, __HIP_MEMORY_SCOPE_AGENT);
        if (v >= (unsigned)NMT) break;
        __builtin_amdgcn_s_sleep(2);
      }
      if (tid < 120) {
        const int e = mt64 * 120 + tid;
        const int hd = e / 960, rem = e - hd * 960, d = rem / 48, f = rem - d * 48;
        float s = 0.f;
        if (f < 40) {
          const float* src = (const float*)(ws + WS_KVP) + (size_t)(c * 4 + hd) * 800 + f * 20 + d;
          float v[NMT];
#pragma unroll
          for (int m = 0; m < NMT; ++m)
            asm volatile("global_load_dword %0, %1, off sc1" : "=v"(v[m]) : "v"(src + (size_t)m * 32 * 800) : "memory");
          asm volatile("s_waitcnt vmcnt(0)" ::: "memory");
#pragma unroll
          for (int m = 0; m < NMT; ++m) { asm volatile("" : "+v"(v[m])); s += v[m]; }
        }
        ((u16*)(ws + WS_KVT))[c * 3840 + e] = f2bf(s);
      }
    }
    STAMP(6);
  } else {
    const int cw = wid - 4, hd = cw;
    bf16x8 wa[2];
    {
      const int hh = (r >> 2) & 1, ii = (r & 3) + 4 * (r >> 3);
      const u16* wsrc = wphi + (size_t)(c * 4 + hd) * 324 + 9 * hh + ii;
#pragma unroll
      for (int s = 0; s < 2; ++s) {
        s16x8 v;
#pragma unroll
        for (int j = 0; j < 8; ++j) {
          const int d = 16 * s + 8 * h + j;
          v[j] = (ii < 9 && d < 18) ? (short)wsrc[d * 18] : (short)0;
        }
        wa[s] = __builtin_bit_cast(bf16x8, v);
      }
    }
    bar();
    issue_qkv(3); issue_qkv(4);
    for (int kc = 0; kc < 9; ++kc) {
      if (kc == 0) { WAITV(28); } else if (kc <= 5) { WAITV(21); } else if (kc == 6) { WAITV(14); } else if (kc == 7) { WAITV(7); } else { WAITV(0); }
      bar();
      if (kc >= 1 && kc + 4 < 9) issue_qkv(kc + 4);
    }
    bar();
    for (int i = tid - 256; i < 576; i += 256) ((float*)(smem + L_B1))[i] = bf2f(b1[c * FC + i]);
    bar();
    const int e0 = cw < 2 ? cw * 5 : 10 + (cw - 2) * 4;
    const bool five = cw < 2;
    unsigned loff[5];
#pragma unroll
    for (int e = 0; e < 5; ++e) {
      int pp = (e0 + e) * 64 + lane, row = pp / 72, ch = pp % 72;
      loff[e] = (unsigned)(row * D_ + ((ch ^ (((row >> 1) & 1) << 2)) * 8)) * 2u;
    }
    auto issue_w2 = [&](int tau, int e) {
      const u16* g = w2 + (size_t)(c * FC + (tau - 8) * 16) * D_;
      glds16(g, loff[e], lds0 + L_W2 + (tau & 3) * 18432 + (e0 + e) * 1024);
    };
    *(u16*)(smem + L_QT + lane * 784 + (256 + hd * 32 + 18) * 2) = 0x3f80;
#pragma unroll 1
    for (int qk = 0; qk < 2; ++qk) {
#pragma unroll
      for (int mt = 0; mt < 2; ++mt) {
        const char* src = smem + L_QT + (mt * 32 + r) * 784 + (qk * 128 + hd * 32 + 8 * h) * 2;
        bf16x8 b0 = *(const bf16x8*)(src);
        u32x4 b1r = *(const u32x4*)(src + 32);
        b1r[0] = h ? 0u : b1r[0]; b1r[1] = 0u; b1r[2] = 0u; b1r[3] = 0u;
        f32x16 d16;
#pragma unroll
        for (int i = 0; i < 16; ++i) d16[i] = 0.f;
        d16 = MFMA32(wa[0], b0, d16);
        d16 = MFMA32(wa[1], __builtin_bit_cast(bf16x8, b1r), d16);
        float pv[9], mx = -3.0e38f, mn = 3.0e38f;
#pragma unroll
        for (int i = 0; i < 9; ++i) { pv[i] = rbf(d16[i]); mx = fmaxf(mx, pv[i]); mn = fminf(mn, pv[i]); }
        mx = fmaxf(mx, __shfl_xor(mx, 32)); mn = fminf(mn, __shfl_xor(mn, 32));
        float ep[9], en[9], sp = 0.f, sn = 0.f;
#pragma unroll
        for (int i = 0; i < 9; ++i) {
          ep[i] = __expf(pv[i] - mx); sp += ep[i];
          en[i] = __expf(mn - pv[i]); sn += en[i];
        }
        sp += __shfl_xor(sp, 32); sn += __shfl_xor(sn, 32);
#pragma unroll
        for (int i = 0; i < 9; ++i) { ep[i] = fdiv(ep[i], sp); en[i] = fdiv(en[i], sn); }
        unsigned o[10];
#pragma unroll
        for (int i = 0; i < 4; ++i) { o[i] = pack2(ep[2 * i], ep[2 * i + 1]); o[5 + i] = pack2(en[2 * i + 1], en[2 * i + 2]); }
        o[4] = pack2(ep[8], en[0]); o[9] = 0u;
        if (qk == 0) {
          char* dst = ws + WS_PHIQ + ((size_t)(mt64 * 2 + mt) * 32 + c * 4 + hd) * 2560 + r * 16;
          if (h == 0) {
            u32x4 v0 = {o[0], o[1], o[2], o[3]}, v1 = {o[4], o[5], o[6], o[7]}; u32x2 v2 = {o[8], o[9]};
            *(u32x4*)(dst) = v0; *(u32x4*)(dst + 512) = v1; *(u32x2*)(dst + 1024) = v2;
          } else {
            u32x2 v0 = {o[0], o[1]}; u32x4 v1 = {o[2], o[3], o[4], o[5]}, v2 = {o[6], o[7], o[8], o[9]};
            *(u32x2*)(dst + 1024 + 8) = v0; *(u32x4*)(dst + 1536) = v1; *(u32x4*)(dst + 2048) = v2;
          }
        } else {
          u32x2* dst = (u32x2*)(smem + L_PK + (hd * 64 + mt * 32 + r) * 96 + h * 40);
#pragma unroll
          for (int i = 0; i < 5; ++i) { u32x2 v = {o[2 * i], o[2 * i + 1]}; dst[i] = v; }
        }
      }
      bar();
    }
    {
      f32x16 kd[2];
#pragma unroll
      for (int t = 0; t < 2; ++t)
#pragma unroll
        for (int i = 0; i < 16; ++i) kd[t][i] = 0.f;
      const char* pa = smem + L_PK + (hd * 64 + 8 * h + q) * 96 + (16 * blk + 4 * p) * 2;
      const char* pb = smem + L_QT + (8 * h + q) * 784 + (256 + hd * 32 + 16 * blk + 4 * p) * 2;
#pragma unroll
      for (int s = 0; s < 4; ++s) {
        bf16x8 bv = cat8(tr_read(pb, s * 16 * 784), tr_read(pb, s * 16 * 784 + 4 * 784));
#pragma unroll
        for (int t = 0; t < 2; ++t) {
          bf16x8 av = cat8(tr_read(pa, s * 16 * 96 + t * 64), tr_read(pa, s * 16 * 96 + 4 * 96 + t * 64));
          kd[t] = MFMA32(av, bv, kd[t]);
        }
      }
      float* kp = (float*)(ws + WS_KVP) + ((size_t)mt64 * 32 + c * 4 + hd) * 800;
      if (r < 20) {
#pragma unroll
        for (int t = 0; t < 2; ++t)
#pragma unroll
          for (int i = 0; i < 16; ++i) {
            int f = t * 32 + (i & 3) + 8 * (i >> 2) + 4 * h;
            if (f < 40) __hip_atomic_store(kp + f * 20 + r, r < 19 ? kd[t][i] : 0.f, __ATOMIC_RELAXED, __HIP_MEMORY_SCOPE_AGENT);
          }
      }
      bar();
    }
    bar();
    waitv0();
    bar();
    if (tid == 256)
      __hip_atomic_fetch_add((unsigned*)(ws + WS_CNT) + c * 16, 1u, __ATOMIC_RELAXED, __HIP_MEMORY_SCOPE_AGENT);
    waitv0();
    const int cmt = cw >> 1, nh = cw & 1;
    const int sw = (q >> 1) & 1;
    const int rowoff = (4 * h + q) * 1152 + nh * 576 + 16 * (2 * blk + (p >> 1)) + 8 * (p & 1);
    const char* wbA = smem + L_W2 + rowoff + (nh ? -64 * sw : 64 * sw);
    const char* wbB = smem + L_W2 + rowoff + (nh ? 64 * sw : -64 * sw);
    char* xown = smem + L_X + (cmt * 2 + nh) * 2048 + lane * 16;
    auto gelu_frag = [&](u32x4 v) {
      u32x4 o;
#pragma unroll
      for (int k = 0; k < 4; ++k) o[k] = pack2(gelu_tanh(lo_bf(v[k])), gelu_tanh(hi_bf(v[k])));
      return o;
    };
#pragma unroll
    for (int tau = 5; tau < 8; ++tau) {
#pragma unroll
      for (int e = 0; e < 4; ++e) issue_w2(tau + 3, e);
      if (five) issue_w2(tau + 3, 4);
      if (tau < 7) {
        *(u32x4*)(xown + (tau - 5) * 1024) = gelu_frag(*(const u32x4*)(xown + (tau - 5) * 1024));
        if (five) { WAITV(5); } else { WAITV(4); }
        bar();
      }
    }
    f32x16 acc[9];
#pragma unroll
    for (int n = 0; n < 9; ++n)
#pragma unroll
      for (int i = 0; i < 16; ++i) acc[n][i] = 0.f;
    bf16x8 xf[4], fr[9];
#pragma unroll
    for (int u = 0; u < 4; ++u)
      xf[u] = *(const bf16x8*)(smem + L_X + (cmt * 2 + (u >> 1)) * 2048 + (u & 1) * 1024 + lane * 16);
#pragma unroll
    for (int n = 0; n < 9; ++n) {
      const char* wb = (n & 1) ? wbB : wbA;
      fr[n] = cat8(tr_read(wb, n * 64), tr_read(wb, n * 64 + 8 * 1152));
    }
    if (five) { WAITV(5); } else { WAITV(4); }
    bar();
    for (int it = 2; it < 11; ++it) {
#pragma unroll
      for (int qd = 0; qd < 4; ++qd) {
        const int tau = it * 4 + qd;
        const bool ld = tau + 3 < 44;
        CSTAMP(qd * 3);
        const int nq = ((qd + 1) & 3) * 18432;
        const bool dog = (qd == 1 || qd == 2) && it <= 9;
        u32x4 rv; f32x2 y[4], t[4];
        if (qd == 1 || qd == 2) rv = *(const u32x4*)(xown + (qd - 1) * 1024);
#pragma unroll
        for (int n = 0; n < 9; ++n) {
          const char* wb = (n & 1) ? wbB : wbA;
          acc[n] = MFMA32(fr[n], xf[qd], acc[n]);
          fr[n] = cat8(tr_read(wb, nq + n * 64), tr_read(wb, nq + n * 64 + 8 * 1152));
          if (ld && n < 4) issue_w2(tau + 3, n);
          if (qd == 1 || qd == 2) {
#pragma unroll
            for (int k = 0; k < 4; ++k) {
              if (n == 1) { f32x2 v = {lo_bf(rv[k]), hi_bf(rv[k])}; y[k] = v; }
              if (n == 2) { f32x2 c1 = {-0.10294324f, -0.10294324f}, c0 = {-2.30220820f, -2.30220820f}; t[k] = (y[k] * y[k]) * c1 + c0; }
              if (n == 3) { t[k] = t[k] * y[k]; }
              if (n == 4) { t[k][0] = __builtin_amdgcn_exp2f(t[k][0]); t[k][1] = __builtin_amdgcn_exp2f(t[k][1]); }
              if (n == 5) { f32x2 one = {1.0f, 1.0f}; t[k] = t[k] + one; }
              if (n == 6) { t[k][0] = __builtin_amdgcn_rcpf(t[k][0]); t[k][1] = __builtin_amdgcn_rcpf(t[k][1]); }
              if (n == 7) { f32x2 gg = y[k] * t[k]; rv[k] = pack2(gg[0], gg[1]); asm volatile("" : "+v"(rv[k])); }
            }
            __builtin_amdgcn_sched_barrier(0);
          }
        }
        if (dog) *(u32x4*)(xown + (qd - 1) * 1024) = rv;
        if (ld && five) issue_w2(tau + 3, 4);
        CSTAMP(qd * 3 + 1);
        if (qd == 3) {
#pragma unroll
          for (int u = 0; u < 4; ++u)
            xf[u] = *(const bf16x8*)(smem + L_X + (cmt * 2 + (u >> 1)) * 2048 + (u & 1) * 1024 + lane * 16);
        }
        CSTAMP(qd * 3 + 2);
        if (!ld) { WAITV(0); } else if (five) { WAITV(5); } else { WAITV(4); }
        TBAR();
      }
    }
    STAMP(5);
    {
      char* img = smem + cw * 18944;
#pragma unroll
      for (int n = 0; n < 9; ++n)
#pragma unroll
        for (int g = 0; g < 4; ++g) {
          u32x2 v = {pack2(acc[n][4 * g], acc[n][4 * g + 1]), pack2(acc[n][4 * g + 2], acc[n][4 * g + 3])};
          *(u32x2*)(img + r * 592 + (n * 32 + 8 * g + 4 * h) * 2) = v;
        }
      asm volatile("s_waitcnt lgkmcnt(0)" ::: "memory");
      u16* pp = (u16*)(ws + WS_PART) + ((size_t)c * S_ + m0 + cmt * 32) * D_ + nh * 288;
#pragma unroll
      for (int i = 0; i < 18; ++i) {
        const int ch = i * 64 + lane, row = ch / 36, cc = ch - row * 36;
        *(u32x4*)(pp + (size_t)row * D_ + cc * 8) = *(const u32x4*)(img + row * 592 + cc * 16);
      }
    }
    STAMP(6);
  }
}

constexpr int L2_WO = 0;
constexpr int L2_O  = 82944;
constexpr int LDS2  = L2_O + 64 * 1168;

__global__ __launch_bounds__(512, 2)
void k2(const u16* __restrict__ x, const u16* __restrict__ b2, char* __restrict__ ws, u16* __restrict__ out) {
  extern __shared__ __attribute__((aligned(16))) char smem[];
  const unsigned lds0 = (unsigned)(size_t)smem;
  const int tid = threadIdx.x, lane = tid & 63;
  const int wid = __builtin_amdgcn_readfirstlane(tid >> 6);
  const int rt = blockIdx.x >> 3, ct = blockIdx.x & 7;
  const int r = lane & 31, h = lane >> 5;
  const int row0 = rt * 64, col0 = ct * 72;

  STAMP2(0);
  {
    const char* g = ws + WS_WOT + (size_t)col0 * D_ * 2;
    for (int e = wid; e < 81; e += 8) {
      const int pp = e * 64 + lane, row = pp / 72, ch = pp - row * 72;
      glds16(g, (unsigned)(row * 1152 + ((ch ^ (row & 7)) * 16)), lds0 + L2_WO + e * 1024);
    }
  }
  STAMP2(1);
  u32x4 aq[4][2][3];
  bf16x8 bk[4][3];
#pragma unroll
  for (int hh = 0; hh < 4; ++hh) {
    const int hd = wid * 4 + hh;
#pragma unroll
    for (int s = 0; s < 3; ++s) {
      bk[hh][s] = *(const bf16x8*)(ws + WS_KVT + ((size_t)(hd * 20 + r) * 48 + 16 * s + 8 * h) * 2);
#pragma unroll
      for (int mt = 0; mt < 2; ++mt)
        aq[hh][mt][s] = *(const u32x4*)(ws + WS_PHIQ + ((size_t)(rt * 2 + mt) * 32 + hd) * 2560 + (s == 2 ? 4 : 2 * s + h) * 512 + r * 16);
    }
  }
  u32x4 pt[2][NCH];
#pragma unroll
  for (int k = 0; k < 2; ++k) {
    const int itx = (k == 0 || tid < 64) ? tid + 512 * k : tid;
    const int row = itx / 9, cg = itx - row * 9;
    const u16* src = (const u16*)(ws + WS_PART) + (size_t)(row0 + row) * D_ + col0 + cg * 8;
#pragma unroll
    for (int cc = 0; cc < NCH; ++cc)
      if (k == 0 || wid == 0) pt[k][cc] = *(const u32x4*)(src + (size_t)cc * S_ * D_);
  }
  STAMP2(2);
#pragma unroll
  for (int hh = 0; hh < 4; ++hh) {
    const int hd = wid * 4 + hh;
#pragma unroll
    for (int mt = 0; mt < 2; ++mt) {
      f32x16 d16;
#pragma unroll
      for (int i = 0; i < 16; ++i) d16[i] = 0.f;
#pragma unroll
      for (int s = 0; s < 3; ++s) {
        u32x4 av = aq[hh][mt][s];
        if (s == 2) { av[0] = h ? 0u : av[0]; av[1] = h ? 0u : av[1]; av[2] = h ? 0u : av[2]; av[3] = h ? 0u : av[3]; }
        d16 = MFMA32(bk[hh][s], __builtin_bit_cast(bf16x8, av), d16);
      }
      float den = __shfl(d16[10], r);
      den = rbf(rbf(den) + 1e-6f);
      const float rd = __builtin_amdgcn_rcpf(den);
      char* orow = smem + L2_O + (mt * 32 + r) * 1168 + (hd * 18 + 4 * h) * 2;
#pragma unroll
      for (int i = 0; i < 10; i += 2) {
        if (i < 8 || h == 0) {
          float a = rbf(d16[i]), b = rbf(d16[i + 1]);
          float qa = a * rd, qb = b * rd;
          qa = fmaf(fmaf(-qa, den, a), rd, qa); qb = fmaf(fmaf(-qb, den, b), rd, qb);
          *(unsigned*)(orow + ((i & 3) + 8 * (i >> 2)) * 2) = pack2(qa, qb);
        }
      }
    }
  }
  STAMP2(3);
  waitv0();
  bar();
  STAMP2(4);
  const int mt4 = wid & 3, ng = wid >> 2;
  const int fr = lane & 15, fq = lane >> 4;
  f32x4 sa[3];
#pragma unroll
  for (int n = 0; n < 3; ++n) sa[n] = (f32x4){0.f, 0.f, 0.f, 0.f};
  const int nc0 = ng ? 48 : 0, nc1 = ng ? 56 : 16, nc2 = 32;
#pragma unroll 6
  for (int ks = 0; ks < 18; ++ks) {
    bf16x8 av = *(const bf16x8*)(smem + L2_O + (mt4 * 16 + fr) * 1168 + (32 * ks + 8 * fq) * 2);
    {
      const int n = nc0 + fr;
      bf16x8 bv = *(const bf16x8*)(smem + L2_WO + n * 1152 + (((4 * ks + fq) ^ (n & 7)) * 16));
      sa[0] = MFMA16(av, bv, sa[0]);
    }
    {
      const int n = nc1 + fr;
      bf16x8 bv = *(const bf16x8*)(smem + L2_WO + n * 1152 + (((4 * ks + fq) ^ (n & 7)) * 16));
      sa[1] = MFMA16(av, bv, sa[1]);
    }
    if (ng == 0) {
      const int n = nc2 + fr;
      bf16x8 bv = *(const bf16x8*)(smem + L2_WO + n * 1152 + (((4 * ks + fq) ^ (n & 7)) * 16));
      sa[2] = MFMA16(av, bv, sa[2]);
    }
  }
  STAMP2(5);
  bar();
  {
    float* st = (float*)(smem + L2_O);
#pragma unroll
    for (int j = 0; j < 4; ++j) {
      const int row = mt4 * 16 + fq * 4 + j;
      st[row * 76 + nc0 + fr] = rbf(sa[0][j]);
      st[row * 76 + nc1 + fr] = rbf(sa[1][j]);
      if (ng == 0) st[row * 76 + nc2 + fr] = rbf(sa[2][j]);
    }
  }
  bar();
#pragma unroll
  for (int k = 0; k < 2; ++k) {
    if (k == 0 || tid < 64) {
      const int itx = tid + 512 * k;
      const int row = itx / 9, cg = itx - row * 9;
      const f32x4 sv0 = *(const f32x4*)(smem + L2_O + (row * 76 + cg * 8) * 4);
      const f32x4 sv1 = *(const f32x4*)(smem + L2_O + (row * 76 + cg * 8 + 4) * 4);
      const size_t go = (size_t)(row0 + row) * D_ + col0 + cg * 8;
      const u32x4 xv = *(const u32x4*)(x + go);
      const u32x4 bv = *(const u32x4*)(b2 + col0 + cg * 8);
      u32x4 ov;
#pragma unroll
      for (int j = 0; j < 4; ++j) {
        float f0 = 0.f, f1 = 0.f;
#pragma unroll
        for (int cc = 0; cc < NCH; ++cc) { f0 += lo_bf(pt[k][cc][j]); f1 += hi_bf(pt[k][cc][j]); }
        f0 = rbf(rbf(f0) + lo_bf(bv[j])); f1 = rbf(rbf(f1) + hi_bf(bv[j]));
        const float g0 = rbf(fdiv(f0, 1.0f + __expf(-f0))), g1 = rbf(fdiv(f1, 1.0f + __expf(-f1)));
        const float s0 = j < 2 ? sv0[2 * j] : sv1[2 * j - 4], s1 = j < 2 ? sv0[2 * j + 1] : sv1[2 * j - 3];
        ov[j] = pack2(lo_bf(xv[j]) + rbf(s0 * g0), hi_bf(xv[j]) + rbf(s1 * g1));
      }
      *(u32x4*)(out + go) = ov;
    }
  }
  STAMP2(6);
}

extern "C" void kernel_launch(void* const* d_in, const int* in_sizes, int n_in,
                              void* d_out, int out_size, void* d_ws, size_t ws_size,
                              hipStream_t stream) {
  const u16* x = (const u16*)d_in[0];
  const u16* wqkv = (const u16*)d_in[1];
  const u16* wphi = (const u16*)d_in[2];
  const u16* wo = (const u16*)d_in[3];
  const u16* w1 = (const u16*)d_in[5];
  const u16* b1 = (const u16*)d_in[6];
  const u16* w2 = (const u16*)d_in[7];
  const u16* b2 = (const u16*)d_in[8];
  char* ws = (char*)d_ws;
  (void)hipFuncSetAttribute((const void*)k1, hipFuncAttributeMaxDynamicSharedMemorySize, LDS1);
  (void)hipFuncSetAttribute((const void*)k2, hipFuncAttributeMaxDynamicSharedMemorySize, LDS2);
  (void)hipMemsetAsync(ws + WS_CNT, 0, 1024, stream);
  k1<<<256, 512, LDS1, stream>>>(x, wqkv, wphi, wo, w1, b1, w2, ws);
  k2<<<256, 512, LDS2, stream>>>(x, b2, ws, (u16*)d_out);
}
```

```cpp
#include <hip/hip_runtime.h>
#include <hip/hip_bf16.h>
#include <stdint.h>
#include <type_traits>

typedef __attribute__((ext_vector_type(8))) __bf16 bf16x8;
typedef __attribute__((ext_vector_type(2))) __bf16 bf16x2;
typedef __attribute__((ext_vector_type(4))) short s16x4;
typedef __attribute__((ext_vector_type(8))) short s16x8;
typedef __attribute__((ext_vector_type(16))) float f32x16;
typedef __attribute__((ext_vector_type(4))) float f32x4;
typedef __attribute__((ext_vector_type(2))) float f32x2;
typedef __attribute__((ext_vector_type(4))) unsigned u32x4;
typedef __attribute__((ext_vector_type(2))) unsigned u32x2;
typedef unsigned short u16;
typedef __attribute__((address_space(3))) s16x4* lds_s16x4_p;

#define DI __device__ __forceinline__

constexpr int S_ = 2048, D_ = 576, F_ = 4608;
constexpr int NCH = 8;
constexpr int FC = F_ / NCH;
constexpr int NMT = S_ / 64;

constexpr size_t WS_CNT  = 0;
constexpr size_t WS_KVP  = 1024;
constexpr size_t WS_KVT  = WS_KVP + (size_t)NMT * 32 * 800 * 4;
constexpr size_t WS_PHIQ = WS_KVT + (size_t)32 * 960 * 2 + 4096;
constexpr size_t WS_WOT  = WS_PHIQ + (size_t)S_ * 32 * 40 * 2 + 256;
constexpr size_t WS_PART = WS_WOT + (size_t)D_ * D_ * 2;
constexpr size_t WS_XN   = WS_PART + (size_t)NCH * S_ * D_ * 2;
constexpr size_t WS_WQ   = WS_XN + (size_t)S_ * D_ * 2;

constexpr int L_W1   = 0;
constexpr int L_W2   = 73728;
constexpr int L_QST  = 0;
constexpr int L_XN   = 86016;
constexpr int L_QT   = 73728;
constexpr int L_PK   = L_QT + 64 * 784;
constexpr int L_X    = 147456;
constexpr int L_B1   = 155648;
constexpr int LDS1   = 163840;

DI float bf2f(u16 v) { return __uint_as_float(((unsigned)v) << 16); }
DI float rbf(float f) { return (float)((__bf16)f); }
DI u16 f2bf(float f) { __bf16 b = (__bf16)f; return __builtin_bit_cast(u16, b); }
DI unsigned pack2(float a, float b) {
  f32x2 v = {a, b};
  bf16x2 p = __builtin_convertvector(v, bf16x2);
  return __builtin_bit_cast(unsigned, p);
}
DI float lo_bf(unsigned u) { return __uint_as_float(u << 16); }
DI float hi_bf(unsigned u) { return __uint_as_float(u & 0xffff0000u); }
DI float fdiv(float a, float b) {
  float r = __builtin_amdgcn_rcpf(b);
  float q = a * r;
  return fmaf(fmaf(-q, b, a), r, q);
}

DI void waitv0()  { asm volatile("s_waitcnt vmcnt(0)" ::: "memory"); }
DI void bar() {
  asm volatile("s_waitcnt lgkmcnt(0)" ::: "memory");
  __builtin_amdgcn_s_barrier();
  asm volatile("" ::: "memory");
}
#define TBAR() bar()
#define WAITV(n) asm volatile("s_waitcnt vmcnt(" #n ")" ::: "memory")

DI s16x4 tr_read(const char* base, int off) {
  return __builtin_amdgcn_ds_read_tr16_b64_v4i16((lds_s16x4_p)(base + off));
}
DI bf16x8 cat8(s16x4 a, s16x4 b) {
  s16x8 v = __builtin_shufflevector(a, b, 0, 1, 2, 3, 4, 5, 6, 7);
  return __builtin_bit_cast(bf16x8, v);
}
DI void glds16(const void* base, unsigned voff, unsigned lds) {
  unsigned keep;
  asm volatile("s_mov_b32 %0, m0\n\ts_mov_b32 m0, %3\n\ts_nop 0\n\tglobal_load_lds_dwordx4 %1, %2\n\ts_mov_b32 m0, %0"
               : "=&s"(keep) : "v"(voff), "s"(base), "s"(lds) : "memory");
}
#define MFMA32(a, b, c) __builtin_amdgcn_mfma_f32_32x32x16_bf16((a), (b), (c), 0, 0, 0)
#define MFMA16(a, b, c) __builtin_amdgcn_mfma_f32_16x16x32_bf16((a), (b), (c), 0, 0, 0)

#define STAMP(i)
#define STAMP2(i)
#define CSTAMP(i)

DI float gelu_tanh(float x) {
  float t = fmaf(x * x, -0.10294324f, -2.30220820f);
  float e = __builtin_amdgcn_exp2f(x * t);
  return x * __builtin_amdgcn_rcpf(1.0f + e);
}

__global__ __launch_bounds__(512)
void k0(const u16* __restrict__ x, const u16* __restrict__ wqkv, const u16* __restrict__ wo,
        const u16* __restrict__ w1, const u16* __restrict__ w2, char* __restrict__ ws) {
  const int tid = threadIdx.x, lane = tid & 63;
  const int wid = __builtin_amdgcn_readfirstlane(tid >> 6);
  const int b = blockIdx.x;
  if (wid == 0) {
    const int tok = b * 8 + (lane >> 3), part = lane & 7;
    const u16* xr = x + (size_t)tok * D_ + part * 8;
    u32x4 raw[9];
#pragma unroll
    for (int i = 0; i < 9; ++i) raw[i] = *(const u32x4*)(xr + 64 * i);
    float sum = 0.f;
#pragma unroll
    for (int i = 0; i < 9; ++i)
#pragma unroll
      for (int j = 0; j < 4; ++j) sum += lo_bf(raw[i][j]) + hi_bf(raw[i][j]);
    sum += __shfl_xor(sum, 1); sum += __shfl_xor(sum, 2); sum += __shfl_xor(sum, 4);
    const float mean = sum * (1.0f / 576.0f);
    float vs = 0.f;
#pragma unroll
    for (int i = 0; i < 9; ++i)
#pragma unroll
      for (int j = 0; j < 4; ++j) {
        float a = lo_bf(raw[i][j]) - mean, bq = hi_bf(raw[i][j]) - mean;
        vs += a * a + bq * bq;
      }
    vs += __shfl_xor(vs, 1); vs += __shfl_xor(vs, 2); vs += __shfl_xor(vs, 4);
    const float mb = rbf(mean);
    const float vb = rbf(vs * (1.0f / 576.0f));
    const float sd = rbf(sqrtf(rbf(vb + 1e-5f)));
    const float rs = __builtin_amdgcn_rcpf(sd);
    char* dst = ws + WS_XN + (size_t)(tok >> 5) * 36864 + (tok & 31) * 16;
#pragma unroll
    for (int i = 0; i < 9; ++i) {
      u32x4 o;
#pragma unroll
      for (int j = 0; j < 4; ++j) {
        float a = rbf(lo_bf(raw[i][j]) - mb), bq = rbf(hi_bf(raw[i][j]) - mb);
        float qa = a * rs, qb = bq * rs;
        qa = fmaf(fmaf(-qa, sd, a), rs, qa); qb = fmaf(fmaf(-qb, sd, bq), rs, qb);
        o[j] = pack2(qa, qb);
      }
      *(u32x4*)(dst + (part + 8 * i) * 512) = o;
    }
    if (b == 0 && lane < 32) ((u32x4*)(ws + WS_CNT))[lane] = (u32x4){0u, 0u, 0u, 0u};
  } else {
    const int t = tid - 64;
    const int c = b & 7, pr = b >> 3;
    u32x4 q0, q1;
    {
      const int i0 = t, rw0 = i0 / 28, ch0 = min(i0 - rw0 * 28, 26);
      q0 = *(const u32x4*)(wqkv + (size_t)(pr * 18 + rw0) * 1728 + (ch0 / 9) * 576 + c * 72 + (ch0 % 9) * 8);
      const int i1 = min(t + 448, 503), rw1 = i1 / 28, ch1 = min(i1 - rw1 * 28, 26);
      q1 = *(const u32x4*)(wqkv + (size_t)(pr * 18 + rw1) * 1728 + (ch1 / 9) * 576 + c * 72 + (ch1 % 9) * 8);
    }
    u32x4 wv[6];
#pragma unroll
    for (int k = 0; k < 3; ++k) {
      const int i = min(t + 448 * k, 1295), rw = i / 72, ch = i - rw * 72;
      wv[k] = *(const u32x4*)(w1 + (size_t)(pr * 18 + rw) * F_ + c * FC + ch * 8);
      wv[3 + k] = *(const u32x4*)(w2 + (size_t)(c * FC + pr * 18) * D_ + (size_t)i * 8);
    }
    if (b < 72) {
      const int k0r = b * 8;
      for (int n = t; n < 576; n += 448) {
        unsigned o[4];
#pragma unroll
        for (int j = 0; j < 4; ++j)
          o[j] = (unsigned)wo[(size_t)(k0r + 2 * j) * D_ + n] | ((unsigned)wo[(size_t)(k0r + 2 * j + 1) * D_ + n] << 16);
        u32x4 v = {o[0], o[1], o[2], o[3]};
        *(u32x4*)(ws + WS_WOT + ((size_t)n * D_ + k0r) * 2) = v;
      }
    }
    char* qd = ws + WS_WQ + (size_t)c * 258048 + (size_t)pr * 18 * 448;
    *(u32x4*)(qd + t * 16) = q0;
    if (t + 448 < 504) *(u32x4*)(qd + (t + 448) * 16) = q1;
#pragma unroll
    for (int k = 0; k < 6; ++k) asm volatile("" :: "v"(wv[k]));
  }
}

__global__ __launch_bounds__(512, 2)
void k1(const u16* __restrict__ x, const u16* __restrict__ wqkv, const u16* __restrict__ wphi,
        const u16* __restrict__ wo, const u16* __restrict__ w1, const u16* __restrict__ b1,
        const u16* __restrict__ w2, char* __restrict__ ws) {
  extern __shared__ __attribute__((aligned(16))) char smem[];
  const unsigned lds0 = (unsigned)(size_t)smem;
  const int tid = threadIdx.x, lane = tid & 63;
  const int wid = __builtin_amdgcn_readfirstlane(tid >> 6);
  const int c = blockIdx.x & 7, mt64 = blockIdx.x >> 3, m0 = mt64 * 64;
  const int r = lane & 31, h = lane >> 5;
  const int blk = (lane >> 4) & 1, q = (lane & 15) >> 2, p = lane & 3;
  const int lpar = (wid >> 1) & 1, lhf = wid & 1;

  STAMP(0);
  auto issue_qkv = [&](int kc) {
    const char* g = ws + WS_WQ + (size_t)c * 258048 + (size_t)kc * 28672;
    const unsigned l = lds0 + L_QST + (kc % 5) * 28672 + (wid - 4) * 7 * 1024;
#pragma unroll
    for (int e = 0; e < 7; ++e) glds16(g, (unsigned)(((wid - 4) * 7 + e) * 1024 + lane * 16), l + e * 1024);
  };
  if (wid >= 4) { issue_qkv(0); issue_qkv(1); }
  STAMP(1);

  if (wid < 4) {
    const int pmt = wid >> 1, pj = wid & 1;
    bf16x8 xn[36];
#pragma unroll
    for (int s = 0; s < 36; ++s)
      xn[s] = *(const bf16x8*)(ws + WS_XN + (size_t)(mt64 * 2 + pmt) * 36864 + s * 1024 + lane * 16);
    STAMP(11);
    {
      f32x16 qa[4];
#pragma unroll
      for (int t = 0; t < 4; ++t)
#pragma unroll
        for (int i = 0; i < 16; ++i) qa[t][i] = 0.f;
      const int lb = (8 * h + q) * 448 + (pj * 4) * 64 + blk * 32 + p * 8;
#pragma unroll
      for (int kc = 0; kc < 9; ++kc) {
        if (kc == 0) { waitv0(); STAMP(7); }
        bar();
        if (kc == 0) STAMP(8);
        if (kc == 4) STAMP(9);
        int lbk = lb + L_QST + (kc % 5) * 28672; asm volatile("" : "+v"(lbk));
        const char* bb = smem + lbk;
#pragma unroll
        for (int ks = 0; ks < 4; ++ks) {
#pragma unroll
          for (int t = 0; t < 4; ++t) {
            s16x4 a0 = tr_read(bb, ks * 16 * 448 + t * 64);
            s16x4 a1 = tr_read(bb, ks * 16 * 448 + 4 * 448 + t * 64);
            qa[t] = MFMA32(cat8(a0, a1), xn[kc * 4 + ks], qa[t]);
          }
        }
      }
      STAMP(12);
      bar();
      int lz = lane; asm volatile("" : "+v"(lz));
      const int h = lz >> 5;
      char* qt = smem + L_QT + (pmt * 32 + (lz & 31)) * 784;
#pragma unroll
      for (int t = 0; t < 4; ++t) {
#pragma unroll
        for (int g = 0; g < 4; ++g) {
          if (pj == 0 || t < 2 || (t == 2 && g < 3)) {
#pragma unroll
            for (int e = 0; e < 2; ++e) {
              const int u = (pj * 4 + t) * 32 + 8 * g + 4 * h + 2 * e;
              const int pos = u + 14 * ((u * 3641) >> 16);
              *(unsigned*)(qt + pos * 2) = pack2(qa[t][4 * g + 2 * e], qa[t][4 * g + 2 * e + 1]);
            }
          }
        }
      }
    }
    STAMP(2);
    const int e0 = (wid & 3) < 2 ? (wid & 3) * 5 : 10 + ((wid & 3) - 2) * 4;
    const bool five = (wid & 3) < 2;
    unsigned loff[5];
#pragma unroll
    for (int e = 0; e < 5; ++e)
      loff[e] = (unsigned)(((e0 + e) * 8 + (lane >> 3)) * F_ + (((lane & 7) ^ (((lane >> 4) & 1) << 2)) * 8)) * 2u;
    auto issue_w1 = [&](int tau, int e) {
      const u16* g = w1 + (size_t)(tau & 3) * 144 * F_ + c * FC + (tau >> 2) * 64;
      glds16(g, loff[e], lds0 + L_W1 + (tau & 3) * 18432 + (e0 + e) * 1024);
    };
#pragma unroll
    for (int t = 0; t < 4; ++t) {
#pragma unroll
      for (int e = 0; e < 4; ++e) issue_w1(t, e);
      if (five) issue_w1(t, 4);
    }
    if (five) { WAITV(10); } else { WAITV(8); }
    bar();
    STAMP(3);
    const int lb = (8 * h + q) * 128 + 16 * ((4 * pj + 2 * blk + (p >> 1)) ^ (((q >> 1) & 1) << 2)) + 8 * (p & 1);
    const char* wb = smem + L_W1 + lb;
    char* xdst = smem + L_X + (pmt * 2 + pj) * 2048 + lane * 16;
    f32x16 xa, xp;
#pragma unroll
    for (int i = 0; i < 16; ++i) { xa[i] = 0.f; xp[i] = 0.f; }
    bf16x8 fr[9];
#pragma unroll
    for (int ks = 0; ks < 9; ++ks) fr[ks] = cat8(tr_read(wb, ks * 2048), tr_read(wb, ks * 2048 + 512));
    bar();
    auto iteration = [&](auto DOM, auto DOG, int it, f32x16& xc, const f32x16& xq) {
      constexpr bool doM = decltype(DOM)::value, doG = decltype(DOG)::value;
#pragma unroll
      for (int qd = 0; qd < 4; ++qd) {
        const int tau = it * 4 + qd;
        const bool ld = tau + 4 < 36;
        CSTAMP(qd * 3);
        if (doM && qd == 0) {
          const float* bp = (const float*)(smem + L_B1) + it * 64 + pj * 32 + 4 * h;
#pragma unroll
          for (int g4 = 0; g4 < 4; ++g4) {
            const f32x4 bv = *(const f32x4*)(bp + 8 * g4);
#pragma unroll
            for (int j = 0; j < 4; ++j) xc[4 * g4 + j] = bv[j];
          }
        }
        if (doG && qd == 0) {
          u32x4 v0 = {pack2(xq[0], xq[1]), pack2(xq[2], xq[3]), pack2(xq[4], xq[5]), pack2(xq[6], xq[7])};
          u32x4 v1 = {pack2(xq[8], xq[9]), pack2(xq[10], xq[11]), pack2(xq[12], xq[13]), pack2(xq[14], xq[15])};
          *(u32x4*)(xdst) = v0; *(u32x4*)(xdst + 1024) = v1;
        }
#pragma unroll
        for (int ks = 0; ks < 9; ++ks) {
          if (doM) {
            const int g = qd * 9 + ks;
            const int noff = ((qd + 1) & 3) * 18432 + ks * 2048;
            xc = MFMA32(fr[ks], xn[g], xc);
            fr[ks] = cat8(tr_read(wb, noff), tr_read(wb, noff + 512));
            if (ld && ks < 4) issue_w1(tau + 4, ks);
          }
        }
        if (doM && ld && five) issue_w1(tau + 4, 4);
        CSTAMP(qd * 3 + 1);
        CSTAMP(qd * 3 + 2);
        if (!(doM && ld)) { WAITV(0); } else if (five) { WAITV(10); } else { WAITV(8); }
        TBAR();
      }
    };
    using T_ = std::integral_constant<bool, true>; using F_t = std::integral_constant<bool, false>;
    iteration(T_{}, F_t{}, 0, xa, xp);
#pragma unroll 1
    for (int it = 1; it < 9; it += 2) {
      iteration(T_{}, T_{}, it, xp, xa);
      if (it == 1) STAMP(4);
      iteration(T_{}, T_{}, it + 1, xa, xp);
    }
    iteration(F_t{}, T_{}, 9, xp, xa);
    iteration(F_t{}, F_t{}, 10, xp, xa);
    STAMP(5);
    if (wid < 2) {
      unsigned* cnt = (unsigned*)(ws + WS_CNT) + c * 16;
      for (int spin = 0; spin < (1 << 22); ++spin) {
        unsigned v = __hip_atomic_load(cnt, __ATOMIC_RELAXED, __HIP_MEMORY_SCOPE_AGENT);
        if (v >= (unsigned)NMT) break;
        __builtin_amdgcn_s_sleep(2);
      }
      if (tid < 120) {
        const int e = mt64 * 120 + tid;
        const int hd = e / 960, rem = e - hd * 960, d = rem / 48, f = rem - d * 48;
        float s = 0.f;
        if (f < 40) {
          const float* src = (const float*)(ws + WS_KVP) + (size_t)(c * 4 + hd) * 800 + f * 20 + d;
          float v[NMT];
#pragma unroll
          for (int m = 0; m < NMT; ++m)
            asm volatile("global_load_dword %0, %1, off sc1" : "=v"(v[m]) : "v"(src + (size_t)m * 32 * 800) : "memory");
          asm volatile("s_waitcnt vmcnt(0)" ::: "memory");
#pragma unroll
          for (int m = 0; m < NMT; ++m) { asm volatile("" : "+v"(v[m])); s += v[m]; }
        }
        ((u16*)(ws + WS_KVT))[c * 3840 + e] = f2bf(s);
      }
    }
    STAMP(6);
  } else {
    const int cw = wid - 4, hd = cw;
    bf16x8 wa[2];
    {
      const int hh = (r >> 2) & 1, ii = (r & 3) + 4 * (r >> 3);
      const u16* wsrc = wphi + (size_t)(c * 4 + hd) * 324 + 9 * hh + ii;
#pragma unroll
      for (int s = 0; s < 2; ++s) {
        s16x8 v;
#pragma unroll
        for (int j = 0; j < 8; ++j) {
          const int d = 16 * s + 8 * h + j;
          v[j] = (ii < 9 && d < 18) ? (short)wsrc[d * 18] : (short)0;
        }
        wa[s] = __builtin_bit_cast(bf16x8, v);
      }
    }
    WAITV(7);
    issue_qkv(2); issue_qkv(3); issue_qkv(4);
    for (int kc = 0; kc < 9; ++kc) {
      if (kc == 0) { WAITV(28); } else if (kc <= 5) { WAITV(21); } else if (kc == 6) { WAITV(14); } else if (kc == 7) { WAITV(7); } else { WAITV(0); }
      bar();
      if (kc >= 1 && kc + 4 < 9) issue_qkv(kc + 4);
    }
    bar();
    for (int i = tid - 256; i < 576; i += 256) ((float*)(smem + L_B1))[i] = bf2f(b1[c * FC + i]);
    bar();
    bar();
    const int e0 = cw < 2 ? cw * 5 : 10 + (cw - 2) * 4;
    const bool five = cw < 2;
    unsigned loff[5];
#pragma unroll
    for (int e = 0; e < 5; ++e) {
      int pp = (e0 + e) * 64 + lane, row = pp / 72, ch = pp % 72;
      loff[e] = (unsigned)(row * D_ + ((ch ^ (((row >> 1) & 1) << 2)) * 8)) * 2u;
    }
    auto issue_w2 = [&](int tau, int e) {
      const u16* g = w2 + (size_t)(c * FC + (tau - 8) * 16) * D_;
      glds16(g, loff[e], lds0 + L_W2 + (tau & 3) * 18432 + (e0 + e) * 1024);
    };
    *(u16*)(smem + L_QT + lane * 784 + (256 + hd * 32 + 18) * 2) = 0x3f80;
#pragma unroll 1
    for (int grp = 0; grp < 4; ++grp) {
      const int qk = grp >> 1, mt = grp & 1;
      {
        const char* src = smem + L_QT + (mt * 32 + r) * 784 + (qk * 128 + hd * 32 + 8 * h) * 2;
        bf16x8 b0 = *(const bf16x8*)(src);
        u32x4 b1r = *(const u32x4*)(src + 32);
        b1r[0] = h ? 0u : b1r[0]; b1r[1] = 0u; b1r[2] = 0u; b1r[3] = 0u;
        f32x16 d16;
#pragma unroll
        for (int i = 0; i < 16; ++i) d16[i] = 0.f;
        d16 = MFMA32(wa[0], b0, d16);
        d16 = MFMA32(wa[1], __builtin_bit_cast(bf16x8, b1r), d16);
        float pv[9], mx = -3.0e38f, mn = 3.0e38f;
#pragma unroll
        for (int i = 0; i < 9; ++i) { pv[i] = rbf(d16[i]); mx = fmaxf(mx, pv[i]); mn = fminf(mn, pv[i]); }
        mx = fmaxf(mx, __shfl_xor(mx, 32)); mn = fminf(mn, __shfl_xor(mn, 32));
        float ep[9], en[9], sp = 0.f, sn = 0.f;
#pragma unroll
        for (int i = 0; i < 9; ++i) {
          ep[i] = __expf(pv[i] - mx); sp += ep[i];
          en[i] = __expf(mn - pv[i]); sn += en[i];
        }
        sp += __shfl_xor(sp, 32); sn += __shfl_xor(sn, 32);
        const float rsp = fdiv(1.0f, sp), rsn = fdiv(1.0f, sn);
#pragma unroll
        for (int i = 0; i < 9; ++i) { ep[i] *= rsp; en[i] *= rsn; }
        unsigned o[10];
#pragma unroll
        for (int i = 0; i < 4; ++i) { o[i] = pack2(ep[2 * i], ep[2 * i + 1]); o[5 + i] = pack2(en[2 * i + 1], en[2 * i + 2]); }
        o[4] = pack2(ep[8], en[0]); o[9] = 0u;
        if (qk == 0) {
          char* dst = ws + WS_PHIQ + ((size_t)(mt64 * 2 + mt) * 32 + c * 4 + hd) * 2560 + r * 16;
          if (h == 0) {
            u32x4 v0 = {o[0], o[1], o[2], o[3]}, v1 = {o[4], o[5], o[6], o[7]}; u32x2 v2 = {o[8], o[9]};
            *(u32x4*)(dst) = v0; *(u32x4*)(dst + 512) = v1; *(u32x2*)(dst + 1024) = v2;
          } else {
            u32x2 v0 = {o[0], o[1]}; u32x4 v1 = {o[2], o[3], o[4], o[5]}, v2 = {o[6], o[7], o[8], o[9]};
            *(u32x2*)(dst + 1024 + 8) = v0; *(u32x4*)(dst + 1536) = v1; *(u32x4*)(dst + 2048) = v2;
          }
        } else {
          u32x2* dst = (u32x2*)(smem + L_PK + (hd * 64 + mt * 32 + r) * 88 + h * 40);
#pragma unroll
          for (int i = 0; i < 5; ++i) { u32x2 v = {o[2 * i], o[2 * i + 1]}; dst[i] = v; }
        }
      }
      STAMP(7 + grp);
      bar();
    }
    {
      f32x16 kd[2];
#pragma unroll
      for (int t = 0; t < 2; ++t)
#pragma unroll
        for (int i = 0; i < 16; ++i) kd[t][i] = 0.f;
      const char* pa = smem + L_PK + (hd * 64 + 8 * h + q) * 88 + (16 * blk + 4 * p) * 2;
      const char* pb = smem + L_QT + (8 * h + q) * 784 + (256 + hd * 32 + 16 * blk + 4 * p) * 2;
#pragma unroll
      for (int s = 0; s < 4; ++s) {
        bf16x8 bv = cat8(tr_read(pb, s * 16 * 784), tr_read(pb, s * 16 * 784 + 4 * 784));
#pragma unroll
        for (int t = 0; t < 2; ++t) {
          bf16x8 av = cat8(tr_read(pa, s * 16 * 88 + t * 64), tr_read(pa, s * 16 * 88 + 4 * 88 + t * 64));
          kd[t] = MFMA32(av, bv, kd[t]);
        }
      }
      float* kp = (float*)(ws + WS_KVP) + ((size_t)mt64 * 32 + c * 4 + hd) * 800;
      if (r < 20) {
#pragma unroll
        for (int t = 0; t < 2; ++t)
#pragma unroll
          for (int i = 0; i < 16; ++i) {
            int f = t * 32 + (i & 3) + 8 * (i >> 2) + 4 * h;
            if (f < 40) __hip_atomic_store(kp + f * 20 + r, r < 19 ? kd[t][i] : 0.f, __ATOMIC_RELAXED, __HIP_MEMORY_SCOPE_AGENT);
          }
      }
      STAMP(11);
      waitv0();
      STAMP(12);
      bar();
    }
    if (tid == 256)
      __hip_atomic_fetch_add((unsigned*)(ws + WS_CNT) + c * 16, 1u, __ATOMIC_RELAXED, __HIP_MEMORY_SCOPE_AGENT);
    waitv0();
    const int cmt = cw >> 1, nh = cw & 1;
    const int sw = (q >> 1) & 1;
    const int rowoff = (4 * h + q) * 1152 + nh * 576 + 16 * (2 * blk + (p >> 1)) + 8 * (p & 1);
    const char* wbA = smem + L_W2 + rowoff + (nh ? -64 * sw : 64 * sw);
    const char* wbB = smem + L_W2 + rowoff + (nh ? 64 * sw : -64 * sw);
    char* xown = smem + L_X + (cmt * 2 + nh) * 2048 + lane * 16;
    auto gelu_frag = [&](u32x4 v) {
      u32x4 o;
#pragma unroll
      for (int k = 0; k < 4; ++k) o[k] = pack2(gelu_tanh(lo_bf(v[k])), gelu_tanh(hi_bf(v[k])));
      return o;
    };
#pragma unroll
    for (int tau = 5; tau < 8; ++tau) {
      if (tau == 5) {
#pragma unroll
        for (int e = 0; e < 4; ++e) issue_w2(8, e);
        if (five) issue_w2(8, 4);
      }
#pragma unroll
      for (int e = 0; e < 4; ++e) issue_w2(tau + 4, e);
      if (five) issue_w2(tau + 4, 4);
      if (tau < 7) {
        *(u32x4*)(xown + (tau - 5) * 1024) = gelu_frag(*(const u32x4*)(xown + (tau - 5) * 1024));
        if (five) { WAITV(10); } else { WAITV(8); }
        bar();
      }
    }
    f32x16 acc[9];
#pragma unroll
    for (int n = 0; n < 9; ++n)
#pragma unroll
      for (int i = 0; i < 16; ++i) acc[n][i] = 0.f;
    bf16x8 xf[4], fr[9];
#pragma unroll
    for (int u = 0; u < 4; ++u)
      xf[u] = *(const bf16x8*)(smem + L_X + (cmt * 2 + (u >> 1)) * 2048 + (u & 1) * 1024 + lane * 16);
#pragma unroll
    for (int n = 0; n < 9; ++n) {
      const char* wb = (n & 1) ? wbB : wbA;
      fr[n] = cat8(tr_read(wb, n * 64), tr_read(wb, n * 64 + 8 * 1152));
    }
    if (five) { WAITV(10); } else { WAITV(8); }
    bar();
    for (int it = 2; it < 11; ++it) {
#pragma unroll
      for (int qd = 0; qd < 4; ++qd) {
        const int tau = it * 4 + qd;
        const bool ld = tau + 4 < 44;
        CSTAMP(qd * 3);
        const int nq = ((qd + 1) & 3) * 18432;
        if ((qd == 1 || qd == 2) && it <= 9) {
          *(u32x4*)(xown + (qd - 1) * 1024) = gelu_frag(*(const u32x4*)(xown + (qd - 1) * 1024));
          __builtin_amdgcn_sched_barrier(0);
        }
#pragma unroll
        for (int n = 0; n < 9; ++n) {
          const char* wb = (n & 1) ? wbB : wbA;
          acc[n] = MFMA32(fr[n], xf[qd], acc[n]);
          fr[n] = cat8(tr_read(wb, nq + n * 64), tr_read(wb, nq + n * 64 + 8 * 1152));
          if (ld && n < 4) issue_w2(tau + 4, n);
        }
        if (ld && five) issue_w2(tau + 4, 4);
        CSTAMP(qd * 3 + 1);
        if (qd == 3) {
#pragma unroll
          for (int u = 0; u < 4; ++u)
            xf[u] = *(const bf16x8*)(smem + L_X + (cmt * 2 + (u >> 1)) * 2048 + (u & 1) * 1024 + lane * 16);
        }
        CSTAMP(qd * 3 + 2);
        if (!ld) { WAITV(0); } else if (five) { WAITV(10); } else { WAITV(8); }
        TBAR();
      }
    }
    STAMP(5);
    {
      char* img = smem + cw * 18944;
#pragma unroll
      for (int n = 0; n < 9; ++n)
#pragma unroll
        for (int g = 0; g < 4; ++g) {
          u32x2 v = {pack2(acc[n][4 * g], acc[n][4 * g + 1]), pack2(acc[n][4 * g + 2], acc[n][4 * g + 3])};
          *(u32x2*)(img + r * 592 + (n * 32 + 8 * g + 4 * h) * 2) = v;
        }
      asm volatile("s_waitcnt lgkmcnt(0)" ::: "memory");
      u16* pp = (u16*)(ws + WS_PART) + ((size_t)c * S_ + m0 + cmt * 32) * D_ + nh * 288;
#pragma unroll
      for (int i = 0; i < 18; ++i) {
        const int ch = i * 64 + lane, row = ch / 36, cc = ch - row * 36;
        *(u32x4*)(pp + (size_t)row * D_ + cc * 8) = *(const u32x4*)(img + row * 592 + cc * 16);
      }
    }
    STAMP(6);
  }
}

constexpr int L2_WO = 0;
constexpr int L2_O  = 82944;
constexpr int LDS2  = L2_O + 64 * 1168;

__global__ __launch_bounds__(512, 2)
void k2(const u16* __restrict__ x, const u16* __restrict__ b2, char* __restrict__ ws, u16* __restrict__ out) {
  extern __shared__ __attribute__((aligned(16))) char smem[];
  const unsigned lds0 = (unsigned)(size_t)smem;
  const int tid = threadIdx.x, lane = tid & 63;
  const int wid = __builtin_amdgcn_readfirstlane(tid >> 6);
  const int rt = (blockIdx.x & 7) * 4 + ((blockIdx.x >> 3) & 3), ct = blockIdx.x >> 5;
  const int r = lane & 31, h = lane >> 5;
  const int row0 = rt * 64, col0 = ct * 72;

  STAMP2(0);
  {
    const char* g = ws + WS_WOT + (size_t)col0 * D_ * 2;
    for (int e = wid; e < 81; e += 8) {
      const int pp = e * 64 + lane, row = pp / 72, ch = pp - row * 72;
      glds16(g, (unsigned)(row * 1152 + ((ch ^ (row & 7)) * 16)), lds0 + L2_WO + e * 1024);
    }
  }
  STAMP2(1);
  u32x4 aq[4][2][3];
  bf16x8 bk[4][3];
#pragma unroll
  for (int hh = 0; hh < 4; ++hh) {
    const int hd = wid * 4 + hh;
#pragma unroll
    for (int s = 0; s < 3; ++s) {
      bk[hh][s] = *(const bf16x8*)(ws + WS_KVT + ((size_t)(hd * 20 + r) * 48 + 16 * s + 8 * h) * 2);
#pragma unroll
      for (int mt = 0; mt < 2; ++mt)
        aq[hh][mt][s] = *(const u32x4*)(ws + WS_PHIQ + ((size_t)(rt * 2 + mt) * 32 + hd) * 2560 + (s == 2 ? 4 : 2 * s + h) * 512 + r * 16);
    }
  }
  u32x4 pt[2][NCH];
#pragma unroll
  for (int k = 0; k < 2; ++k) {
    const int itx = (k == 0 || tid < 64) ? tid + 512 * k : tid;
    const int row = itx / 9, cg = itx - row * 9;
    const u16* src = (const u16*)(ws + WS_PART) + (size_t)(row0 + row) * D_ + col0 + cg * 8;
#pragma unroll
    for (int cc = 0; cc < NCH; ++cc)
      if (k == 0 || wid == 0) pt[k][cc] = *(const u32x4*)(src + (size_t)cc * S_ * D_);
  }
  STAMP2(2);
#pragma unroll
  for (int hh = 0; hh < 4; ++hh) {
    const int hd = wid * 4 + hh;
#pragma unroll
    for (int mt = 0; mt < 2; ++mt) {
      f32x16 d16;
#pragma unroll
      for (int i = 0; i < 16; ++i) d16[i] = 0.f;
#pragma unroll
      for (int s = 0; s < 3; ++s) {
        u32x4 av = aq[hh][mt][s];
        if (s == 2) { av[0] = h ? 0u : av[0]; av[1] = h ? 0u : av[1]; av[2] = h ? 0u : av[2]; av[3] = h ? 0u : av[3]; }
        d16 = MFMA32(bk[hh][s], __builtin_bit_cast(bf16x8, av), d16);
      }
      float den = __shfl(d16[10], r);
      den = rbf(rbf(den) + 1e-6f);
      const float rd = __builtin_amdgcn_rcpf(den);
      char* orow = smem + L2_O + (mt * 32 + r) * 1168 + (hd * 18 + 4 * h) * 2;
#pragma unroll
      for (int i = 0; i < 10; i += 2) {
        if (i < 8 || h == 0) {
          float a = rbf(d16[i]), b = rbf(d16[i + 1]);
          float qa = a * rd, qb = b * rd;
          qa = fmaf(fmaf(-qa, den, a), rd, qa); qb = fmaf(fmaf(-qb, den, b), rd, qb);
          *(unsigned*)(orow + ((i & 3) + 8 * (i >> 2)) * 2) = pack2(qa, qb);
        }
      }
    }
  }
  STAMP2(3);
  waitv0();
  bar();
  STAMP2(4);
  const int mt4 = wid & 3, ng = wid >> 2;
  const int fr = lane & 15, fq = lane >> 4;
  f32x4 sa[3];
#pragma unroll
  for (int n = 0; n < 3; ++n) sa[n] = (f32x4){0.f, 0.f, 0.f, 0.f};
  const int nc0 = ng ? 48 : 0, nc1 = ng ? 56 : 16, nc2 = 32;
  {
    const char* ab = smem + L2_O + (mt4 * 16 + fr) * 1168 + fq * 16;
    const int n0 = nc0 + fr, n1 = nc1 + fr, n2 = nc2 + fr;
    const char* b0e = smem + L2_WO + n0 * 1152 + ((fq ^ (n0 & 7)) * 16);
    const char* b0o = smem + L2_WO + n0 * 1152 + (((4 + fq) ^ (n0 & 7)) * 16);
    const char* b1e = smem + L2_WO + n1 * 1152 + ((fq ^ (n1 & 7)) * 16);
    const char* b1o = smem + L2_WO + n1 * 1152 + (((4 + fq) ^ (n1 & 7)) * 16);
    const char* b2e = smem + L2_WO + n2 * 1152 + ((fq ^ (n2 & 7)) * 16);
    const char* b2o = smem + L2_WO + n2 * 1152 + (((4 + fq) ^ (n2 & 7)) * 16);
#pragma unroll
    for (int ks = 0; ks < 18; ++ks) {
      const int ko = (ks >> 1) * 128;
      bf16x8 av = *(const bf16x8*)(ab + ks * 64);
      sa[0] = MFMA16(av, *(const bf16x8*)(((ks & 1) ? b0o : b0e) + ko), sa[0]);
      sa[1] = MFMA16(av, *(const bf16x8*)(((ks & 1) ? b1o : b1e) + ko), sa[1]);
      sa[2] = MFMA16(av, *(const bf16x8*)(((ks & 1) ? b2o : b2e) + ko), sa[2]);
    }
  }
  STAMP2(5);
  bar();
  {
    float* st = (float*)(smem + L2_O);
#pragma unroll
    for (int j = 0; j < 4; ++j) {
      const int row = mt4 * 16 + fq * 4 + j;
      st[row * 76 + nc0 + fr] = rbf(sa[0][j]);
      st[row * 76 + nc1 + fr] = rbf(sa[1][j]);
      if (ng == 0) st[row * 76 + nc2 + fr] = rbf(sa[2][j]);
    }
  }
  bar();
#pragma unroll
  for (int k = 0; k < 2; ++k) {
    if (k == 0 || tid < 64) {
      const int itx = tid + 512 * k;
      const int row = itx / 9, cg = itx - row * 9;
      const f32x4 sv0 = *(const f32x4*)(smem + L2_O + (row * 76 + cg * 8) * 4);
      const f32x4 sv1 = *(const f32x4*)(smem + L2_O + (row * 76 + cg * 8 + 4) * 4);
      const size_t go = (size_t)(row0 + row) * D_ + col0 + cg * 8;
      const u32x4 xv = *(const u32x4*)(x + go);
      const u32x4 bv = *(const u32x4*)(b2 + col0 + cg * 8);
      u32x4 ov;
#pragma unroll
      for (int j = 0; j < 4; ++j) {
        float f0 = 0.f, f1 = 0.f;
#pragma unroll
        for (int cc = 0; cc < NCH; ++cc) { f0 += lo_bf(pt[k][cc][j]); f1 += hi_bf(pt[k][cc][j]); }
        f0 = rbf(rbf(f0) + lo_bf(bv[j])); f1 = rbf(rbf(f1) + hi_bf(bv[j]));
        const float g0 = rbf(fdiv(f0, 1.0f + __expf(-f0))), g1 = rbf(fdiv(f1, 1.0f + __expf(-f1)));
        const float s0 = j < 2 ? sv0[2 * j] : sv1[2 * j - 4], s1 = j < 2 ? sv0[2 * j + 1] : sv1[2 * j - 3];
        ov[j] = pack2(lo_bf(xv[j]) + rbf(s0 * g0), hi_bf(xv[j]) + rbf(s1 * g1));
      }
      *(u32x4*)(out + go) = ov;
    }
  }
  STAMP2(6);
}

extern "C" void kernel_launch(void* const* d_in, const int* in_sizes, int n_in,
                              void* d_out, int out_size, void* d_ws, size_t ws_size,
                              hipStream_t stream) {
  const u16* x = (const u16*)d_in[0];
  const u16* wqkv = (const u16*)d_in[1];
  const u16* wphi = (const u16*)d_in[2];
  const u16* wo = (const u16*)d_in[3];
  const u16* w1 = (const u16*)d_in[5];
  const u16* b1 = (const u16*)d_in[6];
  const u16* w2 = (const u16*)d_in[7];
  const u16* b2 = (const u16*)d_in[8];
  char* ws = (char*)d_ws;
  (void)hipFuncSetAttribute((const void*)k1, hipFuncAttributeMaxDynamicSharedMemorySize, LDS1);
  (void)hipFuncSetAttribute((const void*)k2, hipFuncAttributeMaxDynamicSharedMemorySize, LDS2);
  k0<<<256, 512, 0, stream>>>(x, wqkv, wo, w1, w2, ws);
  k1<<<256, 512, LDS1, stream>>>(x, wqkv, wphi, wo, w1, b1, w2, ws);
  k2<<<256, 512, LDS2, stream>>>(x, b2, ws, (u16*)d_out);
}
```

```cpp
#include <hip/hip_runtime.h>
#include <hip/hip_bf16.h>
#include <stdint.h>
#include <type_traits>


typedef __attribute__((ext_vector_type(8))) __bf16 bf16x8;
typedef __attribute__((ext_vector_type(2))) __bf16 bf16x2;
typedef __attribute__((ext_vector_type(4))) short s16x4;
typedef __attribute__((ext_vector_type(8))) short s16x8;
typedef __attribute__((ext_vector_type(16))) float f32x16;
typedef __attribute__((ext_vector_type(4))) float f32x4;
typedef __attribute__((ext_vector_type(2))) float f32x2;
typedef __attribute__((ext_vector_type(4))) unsigned u32x4;
typedef __attribute__((ext_vector_type(2))) unsigned u32x2;
typedef unsigned short u16;
typedef __attribute__((address_space(3))) s16x4* lds_s16x4_p;

#define DI __device__ __forceinline__

constexpr int S_ = 2048, D_ = 576, F_ = 4608;
constexpr int NCH = 8;
constexpr int FC = F_ / NCH;
constexpr int NMT = S_ / 64;

constexpr size_t WS_CNT  = 0;
constexpr size_t WS_KVP  = 1024;
constexpr size_t WS_KVT  = WS_KVP + (size_t)NMT * 32 * 800 * 4;
constexpr size_t WS_PHIQ = WS_KVT + (size_t)32 * 960 * 2 + 4096;
constexpr size_t WS_WOT  = WS_PHIQ + (size_t)S_ * 32 * 40 * 2 + 256;
constexpr size_t WS_PART = WS_WOT + (size_t)D_ * D_ * 2;
constexpr size_t WS_XN   = WS_PART + (size_t)NCH * S_ * D_ * 2;
constexpr size_t WS_WQ   = WS_XN + (size_t)S_ * D_ * 2;

constexpr int L_W1   = 0;
constexpr int L_W2   = 73728;
constexpr int L_QST  = 0;
constexpr int L_XN   = 86016;
constexpr int L_QT   = 73728;
constexpr int L_PK   = L_QT + 64 * 784;
constexpr int L_X    = 147456;
constexpr int L_B1   = 155648;
constexpr int LDS1   = 163840;

DI float bf2f(u16 v) { return __uint_as_float(((unsigned)v) << 16); }
DI float rbf(float f) { return (float)((__bf16)f); }
DI u16 f2bf(float f) { __bf16 b = (__bf16)f; return __builtin_bit_cast(u16, b); }
DI unsigned pack2(float a, float b) {
  f32x2 v = {a, b};
  bf16x2 p = __builtin_convertvector(v, bf16x2);
  return __builtin_bit_cast(unsigned, p);
}
DI float lo_bf(unsigned u) { return __uint_as_float(u << 16); }
DI float hi_bf(unsigned u) { return __uint_as_float(u & 0xffff0000u); }
DI float fdiv(float a, float b) {
  float r = __builtin_amdgcn_rcpf(b);
  float q = a * r;
  return fmaf(fmaf(-q, b, a), r, q);
}

DI void waitv0()  { asm volatile("s_waitcnt vmcnt(0)" ::: "memory"); }
DI void bar() {
  asm volatile("s_waitcnt lgkmcnt(0)" ::: "memory");
  __builtin_amdgcn_s_barrier();
  asm volatile("" ::: "memory");
}
#define WAITV(n) asm volatile("s_waitcnt vmcnt(" #n ")" ::: "memory")

DI s16x4 tr_read(const char* base, int off) {
  return __builtin_amdgcn_ds_read_tr16_b64_v4i16((lds_s16x4_p)(base + off));
}
DI bf16x8 cat8(s16x4 a, s16x4 b) {
  s16x8 v = __builtin_shufflevector(a, b, 0, 1, 2, 3, 4, 5, 6, 7);
  return __builtin_bit_cast(bf16x8, v);
}
DI void glds16(const void* base, unsigned voff, unsigned lds) {
  unsigned keep;
  asm volatile("s_mov_b32 %0, m0\n\ts_mov_b32 m0, %3\n\ts_nop 0\n\tglobal_load_lds_dwordx4 %1, %2\n\ts_mov_b32 m0, %0"
               : "=&s"(keep) : "v"(voff), "s"(base), "s"(lds) : "memory");
}
#define MFMA32(a, b, c) __builtin_amdgcn_mfma_f32_32x32x16_bf16((a), (b), (c), 0, 0, 0)
#define MFMA16(a, b, c) __builtin_amdgcn_mfma_f32_16x16x32_bf16((a), (b), (c), 0, 0, 0)

DI float gelu_tanh(float x) {
  float t = fmaf(x * x, -0.10294324f, -2.30220820f);
  float e = __builtin_amdgcn_exp2f(x * t);
  return x * __builtin_amdgcn_rcpf(1.0f + e);
}

__global__ __launch_bounds__(512)
void k0(const u16* __restrict__ x, const u16* __restrict__ wqkv, const u16* __restrict__ wo,
        const u16* __restrict__ w1, const u16* __restrict__ w2, char* __restrict__ ws) {
  const int tid = threadIdx.x, lane = tid & 63;
  const int wid = __builtin_amdgcn_readfirstlane(tid >> 6);
  const int b = blockIdx.x;
  if (wid == 0) {
    const int tok = b * 8 + (lane >> 3), part = lane & 7;
    const u16* xr = x + (size_t)tok * D_ + part * 8;
    u32x4 raw[9];
#pragma unroll
    for (int i = 0; i < 9; ++i) raw[i] = *(const u32x4*)(xr + 64 * i);
    float sum = 0.f;
#pragma unroll
    for (int i = 0; i < 9; ++i)
#pragma unroll
      for (int j = 0; j < 4; ++j) sum += lo_bf(raw[i][j]) + hi_bf(raw[i][j]);
    sum += __shfl_xor(sum, 1); sum += __shfl_xor(sum, 2); sum += __shfl_xor(sum, 4);
    const float mean = sum * (1.0f / 576.0f);
    float vs = 0.f;
#pragma unroll
    for (int i = 0; i < 9; ++i)
#pragma unroll
      for (int j = 0; j < 4; ++j) {
        float a = lo_bf(raw[i][j]) - mean, bq = hi_bf(raw[i][j]) - mean;
        vs += a * a + bq * bq;
      }
    vs += __shfl_xor(vs, 1); vs += __shfl_xor(vs, 2); vs += __shfl_xor(vs, 4);
    const float mb = rbf(mean);
    const float vb = rbf(vs * (1.0f / 576.0f));
    const float sd = rbf(sqrtf(rbf(vb + 1e-5f)));
    const float rs = __builtin_amdgcn_rcpf(sd);
    char* dst = ws + WS_XN + (size_t)(tok >> 5) * 36864 + (tok & 31) * 16;
#pragma unroll
    for (int i = 0; i < 9; ++i) {
      u32x4 o;
#pragma unroll
      for (int j = 0; j < 4; ++j) {
        float a = rbf(lo_bf(raw[i][j]) - mb), bq = rbf(hi_bf(raw[i][j]) - mb);
        float qa = a * rs, qb = bq * rs;
        qa = fmaf(fmaf(-qa, sd, a), rs, qa); qb = fmaf(fmaf(-qb, sd, bq), rs, qb);
        o[j] = pack2(qa, qb);
      }
      *(u32x4*)(dst + (part + 8 * i) * 512) = o;
    }
    if (b == 0 && lane < 32) ((u32x4*)(ws + WS_CNT))[lane] = (u32x4){0u, 0u, 0u, 0u};

  } else {
    const int t = tid - 64;
    const int c = b & 7, pr = b >> 3;
    u32x4 q0, q1;
    {
      const int i0 = t, rw0 = i0 / 28, ch0 = min(i0 - rw0 * 28, 26);
      q0 = *(const u32x4*)(wqkv + (size_t)(pr * 18 + rw0) * 1728 + (ch0 / 9) * 576 + c * 72 + (ch0 % 9) * 8);
      const int i1 = min(t + 448, 503), rw1 = i1 / 28, ch1 = min(i1 - rw1 * 28, 26);
      q1 = *(const u32x4*)(wqkv + (size_t)(pr * 18 + rw1) * 1728 + (ch1 / 9) * 576 + c * 72 + (ch1 % 9) * 8);
    }
    u32x4 wv[6];
#pragma unroll
    for (int k = 0; k < 3; ++k) {
      const int i = min(t + 448 * k, 1295), rw = i / 72, ch = i - rw * 72;
      wv[k] = *(const u32x4*)(w1 + (size_t)(pr * 18 + rw) * F_ + c * FC + ch * 8);
      wv[3 + k] = *(const u32x4*)(w2 + (size_t)(c * FC + pr * 18) * D_ + (size_t)i * 8);
    }
    if (b < 72) {
      const int k0r = b * 8;
      for (int n = t; n < 576; n += 448) {
        unsigned o[4];
#pragma unroll
        for (int j = 0; j < 4; ++j)
          o[j] = (unsigned)wo[(size_t)(k0r + 2 * j) * D_ + n] | ((unsigned)wo[(size_t)(k0r + 2 * j + 1) * D_ + n] << 16);
        u32x4 v = {o[0], o[1], o[2], o[3]};
        *(u32x4*)(ws + WS_WOT + ((size_t)n * D_ + k0r) * 2) = v;
      }
    }
    char* qd = ws + WS_WQ + (size_t)c * 258048 + (size_t)pr * 18 * 448;
    *(u32x4*)(qd + t * 16) = q0;
    if (t + 448 < 504) *(u32x4*)(qd + (t + 448) * 16) = q1;
#pragma unroll
    for (int k = 0; k < 6; ++k) asm volatile("" :: "v"(wv[k]));

  }
}

__global__ __launch_bounds__(512, 2)
void k1(const u16* __restrict__ x, const u16* __restrict__ wqkv, const u16* __restrict__ wphi,
        const u16* __restrict__ wo, const u16* __restrict__ w1, const u16* __restrict__ b1,
        const u16* __restrict__ w2, char* __restrict__ ws) {
  extern __shared__ __attribute__((aligned(16))) char smem[];
  const unsigned lds0 = (unsigned)(size_t)smem;
  const int tid = threadIdx.x, lane = tid & 63;
  const int wid = __builtin_amdgcn_readfirstlane(tid >> 6);
  const int c = blockIdx.x & 7, mt64 = blockIdx.x >> 3, m0 = mt64 * 64;
  const int r = lane & 31, h = lane >> 5;
  const int blk = (lane >> 4) & 1, q = (lane & 15) >> 2, p = lane & 3;
  const int lpar = (wid >> 1) & 1, lhf = wid & 1;
  auto issue_qkv = [&](int kc) {
    const char* g = ws + WS_WQ + (size_t)c * 258048 + (size_t)kc * 28672;
    const unsigned l = lds0 + L_QST + (kc % 5) * 28672 + (wid - 4) * 7 * 1024;
#pragma unroll
    for (int e = 0; e < 7; ++e) glds16(g, (unsigned)(((wid - 4) * 7 + e) * 1024 + lane * 16), l + e * 1024);
  };
  if (wid >= 4) { issue_qkv(0); issue_qkv(1); }
  if (wid < 4) {
    const int pmt = wid >> 1, pj = wid & 1;
    bf16x8 xn[36];
#pragma unroll
    for (int s = 0; s < 36; ++s)
      xn[s] = *(const bf16x8*)(ws + WS_XN + (size_t)(mt64 * 2 + pmt) * 36864 + s * 1024 + lane * 16);
    {
      f32x16 qa[4];
#pragma unroll
      for (int t = 0; t < 4; ++t)
#pragma unroll
        for (int i = 0; i < 16; ++i) qa[t][i] = 0.f;
      const int lb = (8 * h + q) * 448 + (pj * 4) * 64 + blk * 32 + p * 8;
#pragma unroll
      for (int kc = 0; kc < 9; ++kc) {
                bar();
        int lbk = lb + L_QST + (kc % 5) * 28672; asm volatile("" : "+v"(lbk));
        const char* bb = smem + lbk;
#pragma unroll
        for (int ks = 0; ks < 4; ++ks) {
#pragma unroll
          for (int t = 0; t < 4; ++t) {
            s16x4 a0 = tr_read(bb, ks * 16 * 448 + t * 64);
            s16x4 a1 = tr_read(bb, ks * 16 * 448 + 4 * 448 + t * 64);
            qa[t] = MFMA32(cat8(a0, a1), xn[kc * 4 + ks], qa[t]);
          }
        }
      }
      bar();
      int lz = lane; asm volatile("" : "+v"(lz));
      const int h = lz >> 5;
      char* qt = smem + L_QT + (pmt * 32 + (lz & 31)) * 784;
#pragma unroll
      for (int t = 0; t < 4; ++t) {
#pragma unroll
        for (int g = 0; g < 4; ++g) {
          if (pj == 0 || t < 2 || (t == 2 && g < 3)) {
#pragma unroll
            for (int e = 0; e < 2; ++e) {
              const int u = (pj * 4 + t) * 32 + 8 * g + 4 * h + 2 * e;
              const int pos = u + 14 * ((u * 3641) >> 16);
              *(unsigned*)(qt + pos * 2) = pack2(qa[t][4 * g + 2 * e], qa[t][4 * g + 2 * e + 1]);
            }
          }
        }
      }
    }
    const int e0 = (wid & 3) < 2 ? (wid & 3) * 5 : 10 + ((wid & 3) - 2) * 4;
    const bool five = (wid & 3) < 2;
    unsigned loff[5];
#pragma unroll
    for (int e = 0; e < 5; ++e)
      loff[e] = (unsigned)(((e0 + e) * 8 + (lane >> 3)) * F_ + (((lane & 7) ^ (((lane >> 4) & 1) << 2)) * 8)) * 2u;
    auto issue_w1 = [&](int tau, int e) {
      const u16* g = w1 + (size_t)(tau & 3) * 144 * F_ + c * FC + (tau >> 2) * 64;
      glds16(g, loff[e], lds0 + L_W1 + (tau & 3) * 18432 + (e0 + e) * 1024);
    };
#pragma unroll
    for (int t = 0; t < 4; ++t) {
#pragma unroll
      for (int e = 0; e < 4; ++e) issue_w1(t, e);
      if (five) issue_w1(t, 4);
    }
    if (five) { WAITV(10); } else { WAITV(8); }
    bar();
    const int lb = (8 * h + q) * 128 + 16 * ((4 * pj + 2 * blk + (p >> 1)) ^ (((q >> 1) & 1) << 2)) + 8 * (p & 1);
    const char* wb = smem + L_W1 + lb;
    char* xdst = smem + L_X + (pmt * 2 + pj) * 2048 + lane * 16;
    f32x16 xa, xp;
#pragma unroll
    for (int i = 0; i < 16; ++i) { xa[i] = 0.f; xp[i] = 0.f; }
    bf16x8 fr[9];
#pragma unroll
    for (int ks = 0; ks < 9; ++ks) fr[ks] = cat8(tr_read(wb, ks * 2048), tr_read(wb, ks * 2048 + 512));
    bar();
    auto iteration = [&](auto DOM, auto DOG, int it, f32x16& xc, const f32x16& xq) {
      constexpr bool doM = decltype(DOM)::value, doG = decltype(DOG)::value;
#pragma unroll
      for (int qd = 0; qd < 4; ++qd) {
        const int tau = it * 4 + qd;
        const bool ld = tau + 4 < 36;
        if (doM && qd == 0) {
          const float* bp = (const float*)(smem + L_B1) + it * 64 + pj * 32 + 4 * h;
#pragma unroll
          for (int g4 = 0; g4 < 4; ++g4) {
            const f32x4 bv = *(const f32x4*)(bp + 8 * g4);
#pragma unroll
            for (int j = 0; j < 4; ++j) xc[4 * g4 + j] = bv[j];
          }
        }
        if (doG && qd == 0) {
          u32x4 v1 = {pack2(xq[8], xq[9]), pack2(xq[10], xq[11]), pack2(xq[12], xq[13]), pack2(xq[14], xq[15])};
          *(u32x4*)(xdst + 1024) = v1;
        }
        if (doG && qd == 1) {
          u32x4 v0;
#pragma unroll
          for (int k = 0; k < 4; ++k) v0[k] = pack2(gelu_tanh(rbf(xq[2 * k])), gelu_tanh(rbf(xq[2 * k + 1])));
          *(u32x4*)(xdst) = v0;
          __builtin_amdgcn_sched_barrier(0);
        }
#pragma unroll
        for (int ks = 0; ks < 9; ++ks) {
          if (doM) {
            const int g = qd * 9 + ks;
            const int noff = ((qd + 1) & 3) * 18432 + ks * 2048;
            xc = MFMA32(fr[ks], xn[g], xc);
            fr[ks] = cat8(tr_read(wb, noff), tr_read(wb, noff + 512));
            if (ld && ks < 4) issue_w1(tau + 4, ks);
          }
        }
        if (doM && ld && five) issue_w1(tau + 4, 4);
        if (!(doM && ld)) { WAITV(0); } else if (five) { WAITV(10); } else { WAITV(8); }
        bar();
      }
    };
    using T_ = std::integral_constant<bool, true>; using F_t = std::integral_constant<bool, false>;
    iteration(T_{}, F_t{}, 0, xa, xp);
#pragma unroll 1
    for (int it = 1; it < 9; it += 2) {
      iteration(T_{}, T_{}, it, xp, xa);
      iteration(T_{}, T_{}, it + 1, xa, xp);
    }
    iteration(F_t{}, T_{}, 9, xp, xa);
    iteration(F_t{}, F_t{}, 10, xp, xa);
    if (wid < 2) {
      unsigned* cnt = (unsigned*)(ws + WS_CNT) + c * 16;
      for (int spin = 0; spin < (1 << 22); ++spin) {
        unsigned v = __hip_atomic_load(cnt, __ATOMIC_RELAXED, __HIP_MEMORY_SCOPE_AGENT);
        if (v >= (unsigned)NMT) break;
        __builtin_amdgcn_s_sleep(2);
      }
      if (tid < 120) {
        const int e = mt64 * 120 + tid;
        const int hd = e / 960, rem = e - hd * 960, d = rem / 48, f = rem - d * 48;
        float s = 0.f;
        if (f < 40) {
          const float* src = (const float*)(ws + WS_KVP) + (size_t)(c * 4 + hd) * 800 + f * 20 + d;
          float v[NMT];
#pragma unroll
          for (int m = 0; m < NMT; ++m)
            asm volatile("global_load_dword %0, %1, off sc1" : "=v"(v[m]) : "v"(src + (size_t)m * 32 * 800) : "memory");
          asm volatile("s_waitcnt vmcnt(0)" ::: "memory");
#pragma unroll
          for (int m = 0; m < NMT; ++m) { asm volatile("" : "+v"(v[m])); s += v[m]; }
        }
        ((u16*)(ws + WS_KVT))[c * 3840 + e] = f2bf(s);
      }
    }
  } else {
    const int cw = wid - 4, hd = cw;
    bf16x8 wa[2];
    {
      const int hh = (r >> 2) & 1, ii = (r & 3) + 4 * (r >> 3);
      const u16* wsrc = wphi + (size_t)(c * 4 + hd) * 324 + 9 * hh + ii;
#pragma unroll
      for (int s = 0; s < 2; ++s) {
        s16x8 v;
#pragma unroll
        for (int j = 0; j < 8; ++j) {
          const int d = 16 * s + 8 * h + j;
          v[j] = (ii < 9 && d < 18) ? (short)wsrc[d * 18] : (short)0;
        }
        wa[s] = __builtin_bit_cast(bf16x8, v);
      }
    }
    WAITV(7);
    issue_qkv(2); issue_qkv(3); issue_qkv(4);
    for (int kc = 0; kc < 9; ++kc) {
      if (kc == 0) { WAITV(28); } else if (kc <= 5) { WAITV(21); } else if (kc == 6) { WAITV(14); } else if (kc == 7) { WAITV(7); } else { WAITV(0); }
      bar();
      if (kc >= 1 && kc + 4 < 9) issue_qkv(kc + 4);
    }
    bar();
    for (int i = tid - 256; i < 576; i += 256) ((float*)(smem + L_B1))[i] = bf2f(b1[c * FC + i]);
    bar();
    bar();
    const int e0 = cw < 2 ? cw * 5 : 10 + (cw - 2) * 4;
    const bool five = cw < 2;
    unsigned loff[5];
#pragma unroll
    for (int e = 0; e < 5; ++e) {
      int pp = (e0 + e) * 64 + lane, row = pp / 72, ch = pp % 72;
      loff[e] = (unsigned)(row * D_ + ((ch ^ (((row >> 1) & 1) << 2)) * 8)) * 2u;
    }
    auto issue_w2 = [&](int tau, int e) {
      const u16* g = w2 + (size_t)(c * FC + (tau - 8) * 16) * D_;
      glds16(g, loff[e], lds0 + L_W2 + (tau & 3) * 18432 + (e0 + e) * 1024);
    };
    *(u16*)(smem + L_QT + lane * 784 + (256 + hd * 32 + 18) * 2) = 0x3f80;
#pragma unroll 1
    for (int grp = 0; grp < 4; ++grp) {
      const int qk = grp >> 1, mt = grp & 1;
      {
        const char* src = smem + L_QT + (mt * 32 + r) * 784 + (qk * 128 + hd * 32 + 8 * h) * 2;
        bf16x8 b0 = *(const bf16x8*)(src);
        u32x4 b1r = *(const u32x4*)(src + 32);
        b1r[0] = h ? 0u : b1r[0]; b1r[1] = 0u; b1r[2] = 0u; b1r[3] = 0u;
        f32x16 d16;
#pragma unroll
        for (int i = 0; i < 16; ++i) d16[i] = 0.f;
        d16 = MFMA32(wa[0], b0, d16);
        d16 = MFMA32(wa[1], __builtin_bit_cast(bf16x8, b1r), d16);
        float pv[9], mx = -3.0e38f, mn = 3.0e38f;
#pragma unroll
        for (int i = 0; i < 9; ++i) { pv[i] = rbf(d16[i]); mx = fmaxf(mx, pv[i]); mn = fminf(mn, pv[i]); }
        mx = fmaxf(mx, __shfl_xor(mx, 32)); mn = fminf(mn, __shfl_xor(mn, 32));
        float ep[9], en[9], sp = 0.f, sn = 0.f;
#pragma unroll
        for (int i = 0; i < 9; ++i) {
          ep[i] = __expf(pv[i] - mx); sp += ep[i];
          en[i] = __expf(mn - pv[i]); sn += en[i];
        }
        sp += __shfl_xor(sp, 32); sn += __shfl_xor(sn, 32);
        const float rsp = fdiv(1.0f, sp), rsn = fdiv(1.0f, sn);
#pragma unroll
        for (int i = 0; i < 9; ++i) { ep[i] *= rsp; en[i] *= rsn; }
        unsigned o[10];
#pragma unroll
        for (int i = 0; i < 4; ++i) { o[i] = pack2(ep[2 * i], ep[2 * i + 1]); o[5 + i] = pack2(en[2 * i + 1], en[2 * i + 2]); }
        o[4] = pack2(ep[8], en[0]); o[9] = 0u;
        if (qk == 0) {
          char* dst = ws + WS_PHIQ + ((size_t)(mt64 * 2 + mt) * 32 + c * 4 + hd) * 2560 + r * 16;
          if (h == 0) {
            u32x4 v0 = {o[0], o[1], o[2], o[3]}, v1 = {o[4], o[5], o[6], o[7]}; u32x2 v2 = {o[8], o[9]};
            *(u32x4*)(dst) = v0; *(u32x4*)(dst + 512) = v1; *(u32x2*)(dst + 1024) = v2;
          } else {
            u32x2 v0 = {o[0], o[1]}; u32x4 v1 = {o[2], o[3], o[4], o[5]}, v2 = {o[6], o[7], o[8], o[9]};
            *(u32x2*)(dst + 1024 + 8) = v0; *(u32x4*)(dst + 1536) = v1; *(u32x4*)(dst + 2048) = v2;
          }
        } else {
          u32x2* dst = (u32x2*)(smem + L_PK + (hd * 64 + mt * 32 + r) * 88 + h * 40);
#pragma unroll
          for (int i = 0; i < 5; ++i) { u32x2 v = {o[2 * i], o[2 * i + 1]}; dst[i] = v; }
        }
      }
      bar();
    }
    {
      f32x16 kd[2];
#pragma unroll
      for (int t = 0; t < 2; ++t)
#pragma unroll
        for (int i = 0; i < 16; ++i) kd[t][i] = 0.f;
      const char* pa = smem + L_PK + (hd * 64 + 8 * h + q) * 88 + (16 * blk + 4 * p) * 2;
      const char* pb = smem + L_QT + (8 * h + q) * 784 + (256 + hd * 32 + 16 * blk + 4 * p) * 2;
#pragma unroll
      for (int s = 0; s < 4; ++s) {
        bf16x8 bv = cat8(tr_read(pb, s * 16 * 784), tr_read(pb, s * 16 * 784 + 4 * 784));
#pragma unroll
        for (int t = 0; t < 2; ++t) {
          bf16x8 av = cat8(tr_read(pa, s * 16 * 88 + t * 64), tr_read(pa, s * 16 * 88 + 4 * 88 + t * 64));
          kd[t] = MFMA32(av, bv, kd[t]);
        }
      }
      float* kp = (float*)(ws + WS_KVP) + ((size_t)mt64 * 32 + c * 4 + hd) * 800;
      if (r < 20) {
#pragma unroll
        for (int t = 0; t < 2; ++t)
#pragma unroll
          for (int i = 0; i < 16; ++i) {
            int f = t * 32 + (i & 3) + 8 * (i >> 2) + 4 * h;
            if (f < 40) __hip_atomic_store(kp + f * 20 + r, r < 19 ? kd[t][i] : 0.f, __ATOMIC_RELAXED, __HIP_MEMORY_SCOPE_AGENT);
          }
      }
      waitv0();
      bar();
    }
    if (tid == 256)
      __hip_atomic_fetch_add((unsigned*)(ws + WS_CNT) + c * 16, 1u, __ATOMIC_RELAXED, __HIP_MEMORY_SCOPE_AGENT);
    waitv0();
    const int cmt = cw >> 1, nh = cw & 1;
    const int sw = (q >> 1) & 1;
    const int rowoff = (4 * h + q) * 1152 + nh * 576 + 16 * (2 * blk + (p >> 1)) + 8 * (p & 1);
    const char* wbA = smem + L_W2 + rowoff + (nh ? -64 * sw : 64 * sw);
    const char* wbB = smem + L_W2 + rowoff + (nh ? 64 * sw : -64 * sw);
    char* xown = smem + L_X + (cmt * 2 + nh) * 2048 + lane * 16;
    auto gelu_frag = [&](u32x4 v) {
      u32x4 o;
#pragma unroll
      for (int k = 0; k < 4; ++k) o[k] = pack2(gelu_tanh(lo_bf(v[k])), gelu_tanh(hi_bf(v[k])));
      return o;
    };
#pragma unroll
    for (int tau = 5; tau < 8; ++tau) {
      if (tau == 5) {
#pragma unroll
        for (int e = 0; e < 4; ++e) issue_w2(8, e);
        if (five) issue_w2(8, 4);
      }
#pragma unroll
      for (int e = 0; e < 4; ++e) issue_w2(tau + 4, e);
      if (five) issue_w2(tau + 4, 4);
      if (tau < 7) {
        if (tau == 5) *(u32x4*)(xown + 1024) = gelu_frag(*(const u32x4*)(xown + 1024));
        if (five) { WAITV(10); } else { WAITV(8); }
        bar();
      }
    }
    f32x16 acc[9];
#pragma unroll
    for (int n = 0; n < 9; ++n)
#pragma unroll
      for (int i = 0; i < 16; ++i) acc[n][i] = 0.f;
    bf16x8 xf[4], fr[9];
#pragma unroll
    for (int u = 0; u < 4; ++u)
      xf[u] = *(const bf16x8*)(smem + L_X + (cmt * 2 + (u >> 1)) * 2048 + (u & 1) * 1024 + lane * 16);
#pragma unroll
    for (int n = 0; n < 9; ++n) {
      const char* wb = (n & 1) ? wbB : wbA;
      fr[n] = cat8(tr_read(wb, n * 64), tr_read(wb, n * 64 + 8 * 1152));
    }
    if (five) { WAITV(10); } else { WAITV(8); }
    bar();
    for (int it = 2; it < 11; ++it) {
#pragma unroll
      for (int qd = 0; qd < 4; ++qd) {
        const int tau = it * 4 + qd;
        const bool ld = tau + 4 < 44;
        const int nq = ((qd + 1) & 3) * 18432;
        if (qd == 1 && it <= 9) {
          *(u32x4*)(xown + 1024) = gelu_frag(*(const u32x4*)(xown + 1024));
          __builtin_amdgcn_sched_barrier(0);
        }
#pragma unroll
        for (int n = 0; n < 9; ++n) {
          const char* wb = (n & 1) ? wbB : wbA;
          acc[n] = MFMA32(fr[n], xf[qd], acc[n]);
          fr[n] = cat8(tr_read(wb, nq + n * 64), tr_read(wb, nq + n * 64 + 8 * 1152));
          if (ld && n < 4) issue_w2(tau + 4, n);
        }
        if (ld && five) issue_w2(tau + 4, 4);
        if (qd == 3) {
#pragma unroll
          for (int u = 0; u < 4; ++u)
            xf[u] = *(const bf16x8*)(smem + L_X + (cmt * 2 + (u >> 1)) * 2048 + (u & 1) * 1024 + lane * 16);
        }
        if (!ld) { WAITV(0); } else if (five) { WAITV(10); } else { WAITV(8); }
        bar();
      }
    }
    {
      char* img = smem + cw * 18944;
#pragma unroll
      for (int n = 0; n < 9; ++n)
#pragma unroll
        for (int g = 0; g < 4; ++g) {
          u32x2 v = {pack2(acc[n][4 * g], acc[n][4 * g + 1]), pack2(acc[n][4 * g + 2], acc[n][4 * g + 3])};
          *(u32x2*)(img + r * 592 + (n * 32 + 8 * g + 4 * h) * 2) = v;
        }
      asm volatile("s_waitcnt lgkmcnt(0)" ::: "memory");
      u16* pp = (u16*)(ws + WS_PART) + ((size_t)c * S_ + m0 + cmt * 32) * D_ + nh * 288;
#pragma unroll
      for (int i = 0; i < 18; ++i) {
        const int ch = i * 64 + lane, row = ch / 36, cc = ch - row * 36;
        const u32x4 pv = *(const u32x4*)(img + row * 592 + cc * 16);
        *(u32x4*)(pp + (size_t)row * D_ + cc * 8) = pv;
      }
    }
  }
}

constexpr int L2_WO = 0;
constexpr int L2_O  = 82944;
constexpr int LDS2  = L2_O + 64 * 1168;

__global__ __launch_bounds__(512, 2)
void k2(const u16* __restrict__ x, const u16* __restrict__ b2, char* __restrict__ ws, u16* __restrict__ out) {
  extern __shared__ __attribute__((aligned(16))) char smem[];
  const unsigned lds0 = (unsigned)(size_t)smem;
  const int tid = threadIdx.x, lane = tid & 63;
  const int wid = __builtin_amdgcn_readfirstlane(tid >> 6);
  const int rt = (blockIdx.x & 7) * 4 + ((blockIdx.x >> 3) & 3), ct = blockIdx.x >> 5;
  const int r = lane & 31, h = lane >> 5;
  const int row0 = rt * 64, col0 = ct * 72;
  {
    const char* g = ws + WS_WOT + (size_t)col0 * D_ * 2;
    for (int e = wid; e < 81; e += 8) {
      const int pp = e * 64 + lane, row = pp / 72, ch = pp - row * 72;
      glds16(g, (unsigned)(row * 1152 + ((ch ^ (row & 7)) * 16)), lds0 + L2_WO + e * 1024);
    }
  }
  u32x4 aq[4][2][3];
  bf16x8 bk[4][3];
#pragma unroll
  for (int hh = 0; hh < 4; ++hh) {
    const int hd = wid * 4 + hh;
#pragma unroll
    for (int s = 0; s < 3; ++s) {
      bk[hh][s] = *(const bf16x8*)(ws + WS_KVT + ((size_t)(hd * 20 + r) * 48 + 16 * s + 8 * h) * 2);
#pragma unroll
      for (int mt = 0; mt < 2; ++mt)
        aq[hh][mt][s] = *(const u32x4*)(ws + WS_PHIQ + ((size_t)(rt * 2 + mt) * 32 + hd) * 2560 + (s == 2 ? 4 : 2 * s + h) * 512 + r * 16);
    }
  }
  u32x4 pt[2][NCH], xpre[2], bpre[2];
#pragma unroll
  for (int k = 0; k < 2; ++k) {
    const int itx = (k == 0 || tid < 64) ? tid + 512 * k : tid;
    const int row = itx / 9, cg = itx - row * 9;
    const u16* src = (const u16*)(ws + WS_PART) + (size_t)(row0 + row) * D_ + col0 + cg * 8;
#pragma unroll
    for (int cc = 0; cc < NCH; ++cc)
      if (k == 0 || wid == 0) pt[k][cc] = *(const u32x4*)(src + (size_t)cc * S_ * D_);
    if (k == 0 || wid == 0) {
      xpre[k] = *(const u32x4*)(x + (size_t)(row0 + row) * D_ + col0 + cg * 8);
      bpre[k] = *(const u32x4*)(b2 + col0 + cg * 8);
    }
  }
#pragma unroll
  for (int hh = 0; hh < 4; ++hh) {
    const int hd = wid * 4 + hh;
#pragma unroll
    for (int mt = 0; mt < 2; ++mt) {
      f32x16 d16;
#pragma unroll
      for (int i = 0; i < 16; ++i) d16[i] = 0.f;
#pragma unroll
      for (int s = 0; s < 3; ++s) {
        u32x4 av = aq[hh][mt][s];
        if (s == 2) { av[0] = h ? 0u : av[0]; av[1] = h ? 0u : av[1]; av[2] = h ? 0u : av[2]; av[3] = h ? 0u : av[3]; }
        d16 = MFMA32(bk[hh][s], __builtin_bit_cast(bf16x8, av), d16);
      }
      float den = __shfl(d16[10], r);
      den = rbf(rbf(den) + 1e-6f);
      const float rd = __builtin_amdgcn_rcpf(den);
      char* orow = smem + L2_O + (mt * 32 + r) * 1168 + (hd * 18 + 4 * h) * 2;
#pragma unroll
      for (int i = 0; i < 10; i += 2) {
        if (i < 8 || h == 0) {
          float a = rbf(d16[i]), b = rbf(d16[i + 1]);
          float qa = a * rd, qb = b * rd;
          qa = fmaf(fmaf(-qa, den, a), rd, qa); qb = fmaf(fmaf(-qb, den, b), rd, qb);
          *(unsigned*)(orow + ((i & 3) + 8 * (i >> 2)) * 2) = pack2(qa, qb);
        }
      }
    }
  }
  waitv0();
  bar();
  const int mt4 = wid & 3, ng = wid >> 2;
  const int fr = lane & 15, fq = lane >> 4;
  f32x4 sa[3];
#pragma unroll
  for (int n = 0; n < 3; ++n) sa[n] = (f32x4){0.f, 0.f, 0.f, 0.f};
  const int nc0 = ng ? 48 : 0, nc1 = ng ? 56 : 16, nc2 = 32;
  {
    const char* ab = smem + L2_O + (mt4 * 16 + fr) * 1168 + fq * 16;
    const int n0 = nc0 + fr, n1 = nc1 + fr, n2 = nc2 + fr;
    const char* b0e = smem + L2_WO + n0 * 1152 + ((fq ^ (n0 & 7)) * 16);
    const char* b0o = smem + L2_WO + n0 * 1152 + (((4 + fq) ^ (n0 & 7)) * 16);
    const char* b1e = smem + L2_WO + n1 * 1152 + ((fq ^ (n1 & 7)) * 16);
    const char* b1o = smem + L2_WO + n1 * 1152 + (((4 + fq) ^ (n1 & 7)) * 16);
    const char* b2e = smem + L2_WO + n2 * 1152 + ((fq ^ (n2 & 7)) * 16);
    const char* b2o = smem + L2_WO + n2 * 1152 + (((4 + fq) ^ (n2 & 7)) * 16);
#pragma unroll
    for (int ks = 0; ks < 18; ++ks) {
      const int ko = (ks >> 1) * 128;
      bf16x8 av = *(const bf16x8*)(ab + ks * 64);
      sa[0] = MFMA16(av, *(const bf16x8*)(((ks & 1) ? b0o : b0e) + ko), sa[0]);
      sa[1] = MFMA16(av, *(const bf16x8*)(((ks & 1) ? b1o : b1e) + ko), sa[1]);
      sa[2] = MFMA16(av, *(const bf16x8*)(((ks & 1) ? b2o : b2e) + ko), sa[2]);
    }
  }
  bar();
  {
    float* st = (float*)(smem + L2_O);
#pragma unroll
    for (int j = 0; j < 4; ++j) {
      const int row = mt4 * 16 + fq * 4 + j;
      st[row * 76 + nc0 + fr] = rbf(sa[0][j]);
      st[row * 76 + nc1 + fr] = rbf(sa[1][j]);
      if (ng == 0) st[row * 76 + nc2 + fr] = rbf(sa[2][j]);
    }
  }
  bar();
#pragma unroll
  for (int k = 0; k < 2; ++k) {
    if (k == 0 || tid < 64) {
      const int itx = tid + 512 * k;
      const int row = itx / 9, cg = itx - row * 9;
      const f32x4 sv0 = *(const f32x4*)(smem + L2_O + (row * 76 + cg * 8) * 4);
      const f32x4 sv1 = *(const f32x4*)(smem + L2_O + (row * 76 + cg * 8 + 4) * 4);
      const size_t go = (size_t)(row0 + row) * D_ + col0 + cg * 8;
      const u32x4 xv = xpre[k];
      const u32x4 bv = bpre[k];
      u32x4 ov;
#pragma unroll
      for (int j = 0; j < 4; ++j) {
        float f0 = 0.f, f1 = 0.f;
#pragma unroll
        for (int cc = 0; cc < NCH; ++cc) { f0 += lo_bf(pt[k][cc][j]); f1 += hi_bf(pt[k][cc][j]); }
        f0 = rbf(rbf(f0) + lo_bf(bv[j])); f1 = rbf(rbf(f1) + hi_bf(bv[j]));
        const float g0 = rbf(fdiv(f0, 1.0f + __expf(-f0))), g1 = rbf(fdiv(f1, 1.0f + __expf(-f1)));
        const float s0 = j < 2 ? sv0[2 * j] : sv1[2 * j - 4], s1 = j < 2 ? sv0[2 * j + 1] : sv1[2 * j - 3];
        ov[j] = pack2(lo_bf(xv[j]) + rbf(s0 * g0), hi_bf(xv[j]) + rbf(s1 * g1));
      }
      *(u32x4*)(out + go) = ov;
    }
  }
}

extern "C" void kernel_launch(void* const* d_in, const int* in_sizes, int n_in,
                              void* d_out, int out_size, void* d_ws, size_t ws_size,
                              hipStream_t stream) {
  const u16* x = (const u16*)d_in[0];
  const u16* wqkv = (const u16*)d_in[1];
  const u16* wphi = (const u16*)d_in[2];
  const u16* wo = (const u16*)d_in[3];
  const u16* w1 = (const u16*)d_in[5];
  const u16* b1 = (const u16*)d_in[6];
  const u16* w2 = (const u16*)d_in[7];
  const u16* b2 = (const u16*)d_in[8];
  char* ws = (char*)d_ws;
  (void)hipFuncSetAttribute((const void*)k1, hipFuncAttributeMaxDynamicSharedMemorySize, LDS1);
  (void)hipFuncSetAttribute((const void*)k2, hipFuncAttributeMaxDynamicSharedMemorySize, LDS2);
  k0<<<256, 512, 0, stream>>>(x, wqkv, wo, w1, w2, ws);
  k1<<<256, 512, LDS1, stream>>>(x, wqkv, wphi, wo, w1, b1, w2, ws);
  k2<<<256, 512, LDS2, stream>>>(x, b2, ws, (u16*)d_out);
}
```

```cpp
#include <hip/hip_runtime.h>
#include <hip/hip_bf16.h>
#include <stdint.h>
#include <type_traits>


typedef __attribute__((ext_vector_type(8))) __bf16 bf16x8;
typedef __attribute__((ext_vector_type(2))) __bf16 bf16x2;
typedef __attribute__((ext_vector_type(4))) short s16x4;
typedef __attribute__((ext_vector_type(8))) short s16x8;
typedef __attribute__((ext_vector_type(16))) float f32x16;
typedef __attribute__((ext_vector_type(4))) float f32x4;
typedef __attribute__((ext_vector_type(2))) float f32x2;
typedef __attribute__((ext_vector_type(4))) unsigned u32x4;
typedef __attribute__((ext_vector_type(2))) unsigned u32x2;
typedef unsigned short u16;
typedef __attribute__((address_space(3))) s16x4* lds_s16x4_p;

#define DI __device__ __forceinline__

constexpr int S_ = 2048, D_ = 576, F_ = 4608;
constexpr int NCH = 8;
constexpr int FC = F_ / NCH;
constexpr int NMT = S_ / 64;

constexpr size_t WS_CNT  = 0;
constexpr size_t WS_KVP  = 1024;
constexpr size_t WS_KVT  = WS_KVP + (size_t)NMT * 32 * 800 * 4;
constexpr size_t WS_PHIQ = WS_KVT + (size_t)32 * 960 * 2 + 4096;
constexpr size_t WS_WOT  = WS_PHIQ + (size_t)S_ * 32 * 40 * 2 + 256;
constexpr size_t WS_PART = WS_WOT + (size_t)D_ * D_ * 2;
constexpr size_t WS_XN   = WS_PART + (size_t)NCH * S_ * D_ * 2;
constexpr size_t WS_WQ   = WS_XN + (size_t)S_ * D_ * 2;

constexpr int L_W1   = 0;
constexpr int L_W2   = 73728;
constexpr int L_QST  = 0;
constexpr int L_XN   = 86016;
constexpr int L_QT   = 73728;
constexpr int L_PK   = L_QT + 64 * 784;
constexpr int L_X    = 147456;
constexpr int L_B1   = 155648;
constexpr int LDS1   = 163840;

DI float bf2f(u16 v) { return __uint_as_float(((unsigned)v) << 16); }
DI float rbf(float f) { return (float)((__bf16)f); }
DI u16 f2bf(float f) { __bf16 b = (__bf16)f; return __builtin_bit_cast(u16, b); }
DI unsigned pack2(float a, float b) {
  f32x2 v = {a, b};
  bf16x2 p = __builtin_convertvector(v, bf16x2);
  return __builtin_bit_cast(unsigned, p);
}
DI float lo_bf(unsigned u) { return __uint_as_float(u << 16); }
DI float hi_bf(unsigned u) { return __uint_as_float(u & 0xffff0000u); }
DI float fdiv(float a, float b) {
  float r = __builtin_amdgcn_rcpf(b);
  float q = a * r;
  return fmaf(fmaf(-q, b, a), r, q);
}

DI void waitv0()  { asm volatile("s_waitcnt vmcnt(0)" ::: "memory"); }
DI void bar() {
  asm volatile("s_waitcnt lgkmcnt(0)" ::: "memory");
  __builtin_amdgcn_s_barrier();
  asm volatile("" ::: "memory");
}
#define WAITV(n) asm volatile("s_waitcnt vmcnt(" #n ")" ::: "memory")

DI s16x4 tr_read(const char* base, int off) {
  return __builtin_amdgcn_ds_read_tr16_b64_v4i16((lds_s16x4_p)(base + off));
}
DI bf16x8 cat8(s16x4 a, s16x4 b) {
  s16x8 v = __builtin_shufflevector(a, b, 0, 1, 2, 3, 4, 5, 6, 7);
  return __builtin_bit_cast(bf16x8, v);
}
DI void glds16(const void* base, unsigned voff, unsigned lds) {
  unsigned keep;
  asm volatile("s_mov_b32 %0, m0\n\ts_mov_b32 m0, %3\n\ts_nop 0\n\tglobal_load_lds_dwordx4 %1, %2\n\ts_mov_b32 m0, %0"
               : "=&s"(keep) : "v"(voff), "s"(base), "s"(lds) : "memory");
}
#define MFMA32(a, b, c) __builtin_amdgcn_mfma_f32_32x32x16_bf16((a), (b), (c), 0, 0, 0)
#define MFMA16(a, b, c) __builtin_amdgcn_mfma_f32_16x16x32_bf16((a), (b), (c), 0, 0, 0)

DI float gelu_tanh(float x) {
  float t = fmaf(x * x, -0.10294324f, -2.30220820f);
  float e = __builtin_amdgcn_exp2f(x * t);
  return x * __builtin_amdgcn_rcpf(1.0f + e);
}

__global__ __launch_bounds__(512)
void k0(const u16* __restrict__ x, const u16* __restrict__ wqkv, const u16* __restrict__ wo,
        const u16* __restrict__ w1, const u16* __restrict__ w2, char* __restrict__ ws) {
  const int tid = threadIdx.x, lane = tid & 63;
  const int wid = __builtin_amdgcn_readfirstlane(tid >> 6);
  const int b = blockIdx.x;
  if (wid == 0) {
    const int tok = b * 8 + (lane >> 3), part = lane & 7;
    const u16* xr = x + (size_t)tok * D_ + part * 8;
    u32x4 raw[9];
#pragma unroll
    for (int i = 0; i < 9; ++i) raw[i] = *(const u32x4*)(xr + 64 * i);
    float sum = 0.f;
#pragma unroll
    for (int i = 0; i < 9; ++i)
#pragma unroll
      for (int j = 0; j < 4; ++j) sum += lo_bf(raw[i][j]) + hi_bf(raw[i][j]);
    sum += __shfl_xor(sum, 1); sum += __shfl_xor(sum, 2); sum += __shfl_xor(sum, 4);
    const float mean = sum * (1.0f / 576.0f);
    float vs = 0.f;
#pragma unroll
    for (int i = 0; i < 9; ++i)
#pragma unroll
      for (int j = 0; j < 4; ++j) {
        float a = lo_bf(raw[i][j]) - mean, bq = hi_bf(raw[i][j]) - mean;
        vs += a * a + bq * bq;
      }
    vs += __shfl_xor(vs, 1); vs += __shfl_xor(vs, 2); vs += __shfl_xor(vs, 4);
    const float mb = rbf(mean);
    const float vb = rbf(vs * (1.0f / 576.0f));
    const float sd = rbf(sqrtf(rbf(vb + 1e-5f)));
    const float rs = __builtin_amdgcn_rcpf(sd);
    char* dst = ws + WS_XN + (size_t)(tok >> 5) * 36864 + (tok & 31) * 16;
#pragma unroll
    for (int i = 0; i < 9; ++i) {
      u32x4 o;
#pragma unroll
      for (int j = 0; j < 4; ++j) {
        float a = rbf(lo_bf(raw[i][j]) - mb), bq = rbf(hi_bf(raw[i][j]) - mb);
        float qa = a * rs, qb = bq * rs;
        qa = fmaf(fmaf(-qa, sd, a), rs, qa); qb = fmaf(fmaf(-qb, sd, bq), rs, qb);
        o[j] = pack2(qa, qb);
      }
      *(u32x4*)(dst + (part + 8 * i) * 512) = o;
    }
    if (b == 0 && lane < 32) ((u32x4*)(ws + WS_CNT))[lane] = (u32x4){0u, 0u, 0u, 0u};

  } else {
    const int t = tid - 64;
    const int c = b & 7, pr = b >> 3;
    u32x4 q0, q1;
    {
      const int i0 = t, rw0 = i0 / 28, ch0 = min(i0 - rw0 * 28, 26);
      q0 = *(const u32x4*)(wqkv + (size_t)(pr * 18 + rw0) * 1728 + (ch0 / 9) * 576 + c * 72 + (ch0 % 9) * 8);
      const int i1 = min(t + 448, 503), rw1 = i1 / 28, ch1 = min(i1 - rw1 * 28, 26);
      q1 = *(const u32x4*)(wqkv + (size_t)(pr * 18 + rw1) * 1728 + (ch1 / 9) * 576 + c * 72 + (ch1 % 9) * 8);
    }
    if (b < 72) {
      const int k0r = b * 8;
      for (int n = t; n < 576; n += 448) {
        unsigned o[4];
#pragma unroll
        for (int j = 0; j < 4; ++j)
          o[j] = (unsigned)wo[(size_t)(k0r + 2 * j) * D_ + n] | ((unsigned)wo[(size_t)(k0r + 2 * j + 1) * D_ + n] << 16);
        u32x4 v = {o[0], o[1], o[2], o[3]};
        *(u32x4*)(ws + WS_WOT + ((size_t)n * D_ + k0r) * 2) = v;
      }
    }
    char* qd = ws + WS_WQ + (size_t)c * 258048 + (size_t)pr * 18 * 448;
    *(u32x4*)(qd + t * 16) = q0;
    if (t + 448 < 504) *(u32x4*)(qd + (t + 448) * 16) = q1;

  }
}

__global__ __launch_bounds__(512, 2)
void k1(const u16* __restrict__ x, const u16* __restrict__ wqkv, const u16* __restrict__ wphi,
        const u16* __restrict__ wo, const u16* __restrict__ w1, const u16* __restrict__ b1,
        const u16* __restrict__ w2, char* __restrict__ ws) {
  extern __shared__ __attribute__((aligned(16))) char smem[];
  const unsigned lds0 = (unsigned)(size_t)smem;
  const int tid = threadIdx.x, lane = tid & 63;
  const int wid = __builtin_amdgcn_readfirstlane(tid >> 6);
  const int c = blockIdx.x & 7, mt64 = blockIdx.x >> 3, m0 = mt64 * 64;
  const int r = lane & 31, h = lane >> 5;
  const int blk = (lane >> 4) & 1, q = (lane & 15) >> 2, p = lane & 3;
  const int lpar = (wid >> 1) & 1, lhf = wid & 1;
  auto issue_qkv = [&](int kc) {
    const char* g = ws + WS_WQ + (size_t)c * 258048 + (size_t)kc * 28672;
    const unsigned l = lds0 + L_QST + (kc % 5) * 28672 + (wid - 4) * 7 * 1024;
#pragma unroll
    for (int e = 0; e < 7; ++e) glds16(g, (unsigned)(((wid - 4) * 7 + e) * 1024 + lane * 16), l + e * 1024);
  };
  if (wid >= 4) { issue_qkv(0); issue_qkv(1); }
  if (wid < 4) {
    const int pmt = wid >> 1, pj = wid & 1;
    bf16x8 xn[36];
#pragma unroll
    for (int s = 0; s < 36; ++s)
      xn[s] = *(const bf16x8*)(ws + WS_XN + (size_t)(mt64 * 2 + pmt) * 36864 + s * 1024 + lane * 16);
    {
      u32x4 wv[10];
#pragma unroll
      for (int k = 0; k < 5; ++k) {
        const int i = min(tid + 256 * k, 1295), rw = i / 72, ch = i - rw * 72;
        const u16* a1 = w1 + (size_t)(mt64 * 18 + rw) * F_ + c * FC + ch * 8;
        const u16* a2 = w2 + (size_t)(c * FC + mt64 * 18) * D_ + (size_t)i * 8;
        asm volatile("global_load_dwordx4 %0, %1, off" : "=v"(wv[k]) : "v"(a1));
        asm volatile("global_load_dwordx4 %0, %1, off" : "=v"(wv[5 + k]) : "v"(a2));
      }
      waitv0();
#pragma unroll
      for (int k = 0; k < 10; ++k) asm volatile("" :: "v"(wv[k]));
    }
    {
      f32x16 qa[4];
#pragma unroll
      for (int t = 0; t < 4; ++t)
#pragma unroll
        for (int i = 0; i < 16; ++i) qa[t][i] = 0.f;
      const int lb = (8 * h + q) * 448 + (pj * 4) * 64 + blk * 32 + p * 8;
#pragma unroll
      for (int kc = 0; kc < 9; ++kc) {
                bar();
        int lbk = lb + L_QST + (kc % 5) * 28672; asm volatile("" : "+v"(lbk));
        const char* bb = smem + lbk;
#pragma unroll
        for (int ks = 0; ks < 4; ++ks) {
#pragma unroll
          for (int t = 0; t < 4; ++t) {
            s16x4 a0 = tr_read(bb, ks * 16 * 448 + t * 64);
            s16x4 a1 = tr_read(bb, ks * 16 * 448 + 4 * 448 + t * 64);
            qa[t] = MFMA32(cat8(a0, a1), xn[kc * 4 + ks], qa[t]);
          }
        }
      }
      bar();
      int lz = lane; asm volatile("" : "+v"(lz));
      const int h = lz >> 5;
      char* qt = smem + L_QT + (pmt * 32 + (lz & 31)) * 784;
#pragma unroll
      for (int t = 0; t < 4; ++t) {
#pragma unroll
        for (int g = 0; g < 4; ++g) {
          if (pj == 0 || t < 2 || (t == 2 && g < 3)) {
#pragma unroll
            for (int e = 0; e < 2; ++e) {
              const int u = (pj * 4 + t) * 32 + 8 * g + 4 * h + 2 * e;
              const int pos = u + 14 * ((u * 3641) >> 16);
              *(unsigned*)(qt + pos * 2) = pack2(qa[t][4 * g + 2 * e], qa[t][4 * g + 2 * e + 1]);
            }
          }
        }
      }
    }
    const int e0 = (wid & 3) < 2 ? (wid & 3) * 5 : 10 + ((wid & 3) - 2) * 4;
    const bool five = (wid & 3) < 2;
    unsigned loff[5];
#pragma unroll
    for (int e = 0; e < 5; ++e)
      loff[e] = (unsigned)(((e0 + e) * 8 + (lane >> 3)) * F_ + (((lane & 7) ^ (((lane >> 4) & 1) << 2)) * 8)) * 2u;
    auto issue_w1 = [&](int tau, int e) {
      const u16* g = w1 + (size_t)(tau & 3) * 144 * F_ + c * FC + (tau >> 2) * 64;
      glds16(g, loff[e], lds0 + L_W1 + (tau & 3) * 18432 + (e0 + e) * 1024);
    };
#pragma unroll
    for (int t = 0; t < 4; ++t) {
#pragma unroll
      for (int e = 0; e < 4; ++e) issue_w1(t, e);
      if (five) issue_w1(t, 4);
    }
    if (five) { WAITV(10); } else { WAITV(8); }
    bar();
    const int lb = (8 * h + q) * 128 + 16 * ((4 * pj + 2 * blk + (p >> 1)) ^ (((q >> 1) & 1) << 2)) + 8 * (p & 1);
    const char* wb = smem + L_W1 + lb;
    char* xdst = smem + L_X + (pmt * 2 + pj) * 2048 + lane * 16;
    f32x16 xa, xp;
#pragma unroll
    for (int i = 0; i < 16; ++i) { xa[i] = 0.f; xp[i] = 0.f; }
    bf16x8 fr[9];
#pragma unroll
    for (int ks = 0; ks < 9; ++ks) fr[ks] = cat8(tr_read(wb, ks * 2048), tr_read(wb, ks * 2048 + 512));
    bar();
    auto iteration = [&](auto DOM, auto DOG, int it, f32x16& xc, const f32x16& xq) {
      constexpr bool doM = decltype(DOM)::value, doG = decltype(DOG)::value;
#pragma unroll
      for (int qd = 0; qd < 4; ++qd) {
        const int tau = it * 4 + qd;
        const bool ld = tau + 4 < 36;
        if (doM && qd == 0) {
          const float* bp = (const float*)(smem + L_B1) + it * 64 + pj * 32 + 4 * h;
#pragma unroll
          for (int g4 = 0; g4 < 4; ++g4) {
            const f32x4 bv = *(const f32x4*)(bp + 8 * g4);
#pragma unroll
            for (int j = 0; j < 4; ++j) xc[4 * g4 + j] = bv[j];
          }
        }
        if (doG && qd == 0) {
          u32x4 v1 = {pack2(xq[8], xq[9]), pack2(xq[10], xq[11]), pack2(xq[12], xq[13]), pack2(xq[14], xq[15])};
          *(u32x4*)(xdst + 1024) = v1;
        }
        if (doG && qd == 1) {
          u32x4 v0;
#pragma unroll
          for (int k = 0; k < 4; ++k) v0[k] = pack2(gelu_tanh(rbf(xq[2 * k])), gelu_tanh(rbf(xq[2 * k + 1])));
          *(u32x4*)(xdst) = v0;
          __builtin_amdgcn_sched_barrier(0);
        }
#pragma unroll
        for (int ks = 0; ks < 9; ++ks) {
          if (doM) {
            const int g = qd * 9 + ks;
            const int noff = ((qd + 1) & 3) * 18432 + ks * 2048;
            xc = MFMA32(fr[ks], xn[g], xc);
            fr[ks] = cat8(tr_read(wb, noff), tr_read(wb, noff + 512));
            if (ld && ks < 4) issue_w1(tau + 4, ks);
          }
        }
        if (doM && ld && five) issue_w1(tau + 4, 4);
        if (!(doM && ld)) { WAITV(0); } else if (five) { WAITV(10); } else { WAITV(8); }
        bar();
      }
    };
    using T_ = std::integral_constant<bool, true>; using F_t = std::integral_constant<bool, false>;
    iteration(T_{}, F_t{}, 0, xa, xp);
#pragma unroll 1
    for (int it = 1; it < 9; it += 2) {
      iteration(T_{}, T_{}, it, xp, xa);
      iteration(T_{}, T_{}, it + 1, xa, xp);
    }
    iteration(F_t{}, T_{}, 9, xp, xa);
    iteration(F_t{}, F_t{}, 10, xp, xa);
    if (wid < 2) {
      unsigned* cnt = (unsigned*)(ws + WS_CNT) + c * 16;
      for (int spin = 0; spin < (1 << 22); ++spin) {
        unsigned v = __hip_atomic_load(cnt, __ATOMIC_RELAXED, __HIP_MEMORY_SCOPE_AGENT);
        if (v >= (unsigned)NMT) break;
        __builtin_amdgcn_s_sleep(2);
      }
      if (tid < 120) {
        const int e = mt64 * 120 + tid;
        const int hd = e / 960, rem = e - hd * 960, d = rem / 48, f = rem - d * 48;
        float s = 0.f;
        if (f < 40) {
          const float* src = (const float*)(ws + WS_KVP) + (size_t)(c * 4 + hd) * 800 + f * 20 + d;
          float v[NMT];
#pragma unroll
          for (int m = 0; m < NMT; ++m)
            asm volatile("global_load_dword %0, %1, off sc1" : "=v"(v[m]) : "v"(src + (size_t)m * 32 * 800) : "memory");
          asm volatile("s_waitcnt vmcnt(0)" ::: "memory");
#pragma unroll
          for (int m = 0; m < NMT; ++m) { asm volatile("" : "+v"(v[m])); s += v[m]; }
        }
        ((u16*)(ws + WS_KVT))[c * 3840 + e] = f2bf(s);
      }
    }
  } else {
    const int cw = wid - 4, hd = cw;
    bf16x8 wa[2];
    {
      const int hh = (r >> 2) & 1, ii = (r & 3) + 4 * (r >> 3);
      const u16* wsrc = wphi + (size_t)(c * 4 + hd) * 324 + 9 * hh + ii;
#pragma unroll
      for (int s = 0; s < 2; ++s) {
        s16x8 v;
#pragma unroll
        for (int j = 0; j < 8; ++j) {
          const int d = 16 * s + 8 * h + j;
          v[j] = (ii < 9 && d < 18) ? (short)wsrc[d * 18] : (short)0;
        }
        wa[s] = __builtin_bit_cast(bf16x8, v);
      }
    }
    WAITV(7);
    issue_qkv(2); issue_qkv(3); issue_qkv(4);
    for (int kc = 0; kc < 9; ++kc) {
      if (kc == 0) { WAITV(28); } else if (kc <= 5) { WAITV(21); } else if (kc == 6) { WAITV(14); } else if (kc == 7) { WAITV(7); } else { WAITV(0); }
      bar();
      if (kc >= 1 && kc + 4 < 9) issue_qkv(kc + 4);
    }
    bar();
    for (int i = tid - 256; i < 576; i += 256) ((float*)(smem + L_B1))[i] = bf2f(b1[c * FC + i]);
    bar();
    bar();
    const int e0 = cw < 2 ? cw * 5 : 10 + (cw - 2) * 4;
    const bool five = cw < 2;
    unsigned loff[5];
#pragma unroll
    for (int e = 0; e < 5; ++e) {
      int pp = (e0 + e) * 64 + lane, row = pp / 72, ch = pp % 72;
      loff[e] = (unsigned)(row * D_ + ((ch ^ (((row >> 1) & 1) << 2)) * 8)) * 2u;
    }
    auto issue_w2 = [&](int tau, int e) {
      const u16* g = w2 + (size_t)(c * FC + (tau - 8) * 16) * D_;
      glds16(g, loff[e], lds0 + L_W2 + (tau & 3) * 18432 + (e0 + e) * 1024);
    };
    *(u16*)(smem + L_QT + lane * 784 + (256 + hd * 32 + 18) * 2) = 0x3f80;
#pragma unroll 1
    for (int grp = 0; grp < 4; ++grp) {
      const int qk = grp >> 1, mt = grp & 1;
      {
        const char* src = smem + L_QT + (mt * 32 + r) * 784 + (qk * 128 + hd * 32 + 8 * h) * 2;
        bf16x8 b0 = *(const bf16x8*)(src);
        u32x4 b1r = *(const u32x4*)(src + 32);
        b1r[0] = h ? 0u : b1r[0]; b1r[1] = 0u; b1r[2] = 0u; b1r[3] = 0u;
        f32x16 d16;
#pragma unroll
        for (int i = 0; i < 16; ++i) d16[i] = 0.f;
        d16 = MFMA32(wa[0], b0, d16);
        d16 = MFMA32(wa[1], __builtin_bit_cast(bf16x8, b1r), d16);
        float pv[9], mx = -3.0e38f, mn = 3.0e38f;
#pragma unroll
        for (int i = 0; i < 9; ++i) { pv[i] = rbf(d16[i]); mx = fmaxf(mx, pv[i]); mn = fminf(mn, pv[i]); }
        mx = fmaxf(mx, __shfl_xor(mx, 32)); mn = fminf(mn, __shfl_xor(mn, 32));
        float ep[9], en[9], sp = 0.f, sn = 0.f;
#pragma unroll
        for (int i = 0; i < 9; ++i) {
          ep[i] = __expf(pv[i] - mx); sp += ep[i];
          en[i] = __expf(mn - pv[i]); sn += en[i];
        }
        sp += __shfl_xor(sp, 32); sn += __shfl_xor(sn, 32);
        const float rsp = fdiv(1.0f, sp), rsn = fdiv(1.0f, sn);
#pragma unroll
        for (int i = 0; i < 9; ++i) { ep[i] *= rsp; en[i] *= rsn; }
        unsigned o[10];
#pragma unroll
        for (int i = 0; i < 4; ++i) { o[i] = pack2(ep[2 * i], ep[2 * i + 1]); o[5 + i] = pack2(en[2 * i + 1], en[2 * i + 2]); }
        o[4] = pack2(ep[8], en[0]); o[9] = 0u;
        if (qk == 0) {
          char* dst = ws + WS_PHIQ + ((size_t)(mt64 * 2 + mt) * 32 + c * 4 + hd) * 2560 + r * 16;
          if (h == 0) {
            u32x4 v0 = {o[0], o[1], o[2], o[3]}, v1 = {o[4], o[5], o[6], o[7]}; u32x2 v2 = {o[8], o[9]};
            *(u32x4*)(dst) = v0; *(u32x4*)(dst + 512) = v1; *(u32x2*)(dst + 1024) = v2;
          } else {
            u32x2 v0 = {o[0], o[1]}; u32x4 v1 = {o[2], o[3], o[4], o[5]}, v2 = {o[6], o[7], o[8], o[9]};
            *(u32x2*)(dst + 1024 + 8) = v0; *(u32x4*)(dst + 1536) = v1; *(u32x4*)(dst + 2048) = v2;
          }
        } else {
          u32x2* dst = (u32x2*)(smem + L_PK + (hd * 64 + mt * 32 + r) * 88 + h * 40);
#pragma unroll
          for (int i = 0; i < 5; ++i) { u32x2 v = {o[2 * i], o[2 * i + 1]}; dst[i] = v; }
        }
      }
      bar();
    }
    {
      f32x16 kd[2];
#pragma unroll
      for (int t = 0; t < 2; ++t)
#pragma unroll
        for (int i = 0; i < 16; ++i) kd[t][i] = 0.f;
      const char* pa = smem + L_PK + (hd * 64 + 8 * h + q) * 88 + (16 * blk + 4 * p) * 2;
      const char* pb = smem + L_QT + (8 * h + q) * 784 + (256 + hd * 32 + 16 * blk + 4 * p) * 2;
#pragma unroll
      for (int s = 0; s < 4; ++s) {
        bf16x8 bv = cat8(tr_read(pb, s * 16 * 784), tr_read(pb, s * 16 * 784 + 4 * 784));
#pragma unroll
        for (int t = 0; t < 2; ++t) {
          bf16x8 av = cat8(tr_read(pa, s * 16 * 88 + t * 64), tr_read(pa, s * 16 * 88 + 4 * 88 + t * 64));
          kd[t] = MFMA32(av, bv, kd[t]);
        }
      }
      float* kp = (float*)(ws + WS_KVP) + ((size_t)mt64 * 32 + c * 4 + hd) * 800;
      if (r < 20) {
#pragma unroll
        for (int t = 0; t < 2; ++t)
#pragma unroll
          for (int i = 0; i < 16; ++i) {
            int f = t * 32 + (i & 3) + 8 * (i >> 2) + 4 * h;
            if (f < 40) __hip_atomic_store(kp + f * 20 + r, r < 19 ? kd[t][i] : 0.f, __ATOMIC_RELAXED, __HIP_MEMORY_SCOPE_AGENT);
          }
      }
      waitv0();
      bar();
    }
    if (tid == 256)
      __hip_atomic_fetch_add((unsigned*)(ws + WS_CNT) + c * 16, 1u, __ATOMIC_RELAXED, __HIP_MEMORY_SCOPE_AGENT);
    waitv0();
    const int cmt = cw >> 1, nh = cw & 1;
    const int sw = (q >> 1) & 1;
    const int rowoff = (4 * h + q) * 1152 + nh * 576 + 16 * (2 * blk + (p >> 1)) + 8 * (p & 1);
    const char* wbA = smem + L_W2 + rowoff + (nh ? -64 * sw : 64 * sw);
    const char* wbB = smem + L_W2 + rowoff + (nh ? 64 * sw : -64 * sw);
    char* xown = smem + L_X + (cmt * 2 + nh) * 2048 + lane * 16;
    auto gelu_frag = [&](u32x4 v) {
      u32x4 o;
#pragma unroll
      for (int k = 0; k < 4; ++k) o[k] = pack2(gelu_tanh(lo_bf(v[k])), gelu_tanh(hi_bf(v[k])));
      return o;
    };
#pragma unroll
    for (int tau = 5; tau < 8; ++tau) {
      if (tau == 5) {
#pragma unroll
        for (int e = 0; e < 4; ++e) issue_w2(8, e);
        if (five) issue_w2(8, 4);
      }
#pragma unroll
      for (int e = 0; e < 4; ++e) issue_w2(tau + 4, e);
      if (five) issue_w2(tau + 4, 4);
      if (tau < 7) {
        if (tau == 5) *(u32x4*)(xown + 1024) = gelu_frag(*(const u32x4*)(xown + 1024));
        if (five) { WAITV(10); } else { WAITV(8); }
        bar();
      }
    }
    f32x16 acc[9];
#pragma unroll
    for (int n = 0; n < 9; ++n)
#pragma unroll
      for (int i = 0; i < 16; ++i) acc[n][i] = 0.f;
    bf16x8 xf[4], fr[9];
#pragma unroll
    for (int u = 0; u < 4; ++u)
      xf[u] = *(const bf16x8*)(smem + L_X + (cmt * 2 + (u >> 1)) * 2048 + (u & 1) * 1024 + lane * 16);
#pragma unroll
    for (int n = 0; n < 9; ++n) {
      const char* wb = (n & 1) ? wbB : wbA;
      fr[n] = cat8(tr_read(wb, n * 64), tr_read(wb, n * 64 + 8 * 1152));
    }
    if (five) { WAITV(10); } else { WAITV(8); }
    bar();
    for (int it = 2; it < 11; ++it) {
#pragma unroll
      for (int qd = 0; qd < 4; ++qd) {
        const int tau = it * 4 + qd;
        const bool ld = tau + 4 < 44;
        const int nq = ((qd + 1) & 3) * 18432;
        if (qd == 1 && it <= 9) {
          *(u32x4*)(xown + 1024) = gelu_frag(*(const u32x4*)(xown + 1024));
          __builtin_amdgcn_sched_barrier(0);
        }
#pragma unroll
        for (int n = 0; n < 9; ++n) {
          const char* wb = (n & 1) ? wbB : wbA;
          acc[n] = MFMA32(fr[n], xf[qd], acc[n]);
          fr[n] = cat8(tr_read(wb, nq + n * 64), tr_read(wb, nq + n * 64 + 8 * 1152));
          if (ld && n < 4) issue_w2(tau + 4, n);
        }
        if (ld && five) issue_w2(tau + 4, 4);
        if (qd == 3) {
#pragma unroll
          for (int u = 0; u < 4; ++u)
            xf[u] = *(const bf16x8*)(smem + L_X + (cmt * 2 + (u >> 1)) * 2048 + (u & 1) * 1024 + lane * 16);
        }
        if (!ld) { WAITV(0); } else if (five) { WAITV(10); } else { WAITV(8); }
        bar();
      }
    }
    {
      char* img = smem + cw * 18944;
#pragma unroll
      for (int n = 0; n < 9; ++n)
#pragma unroll
        for (int g = 0; g < 4; ++g) {
          u32x2 v = {pack2(acc[n][4 * g], acc[n][4 * g + 1]), pack2(acc[n][4 * g + 2], acc[n][4 * g + 3])};
          *(u32x2*)(img + r * 592 + (n * 32 + 8 * g + 4 * h) * 2) = v;
        }
      asm volatile("s_waitcnt lgkmcnt(0)" ::: "memory");
      u16* pp = (u16*)(ws + WS_PART) + ((size_t)c * S_ + m0 + cmt * 32) * D_ + nh * 288;
#pragma unroll
      for (int i = 0; i < 18; ++i) {
        const int ch = i * 64 + lane, row = ch / 36, cc = ch - row * 36;
        const u32x4 pv = *(const u32x4*)(img + row * 592 + cc * 16);
        *(u32x4*)(pp + (size_t)row * D_ + cc * 8) = pv;
      }
    }
  }
}

constexpr int L2_WO = 0;
constexpr int L2_O  = 82944;
constexpr int LDS2  = L2_O + 64 * 1168;

__global__ __launch_bounds__(512, 2)
void k2(const u16* __restrict__ x, const u16* __restrict__ b2, char* __restrict__ ws, u16* __restrict__ out) {
  extern __shared__ __attribute__((aligned(16))) char smem[];
  const unsigned lds0 = (unsigned)(size_t)smem;
  const int tid = threadIdx.x, lane = tid & 63;
  const int wid = __builtin_amdgcn_readfirstlane(tid >> 6);
  const int rt = (blockIdx.x & 7) * 4 + ((blockIdx.x >> 3) & 3), ct = blockIdx.x >> 5;
  const int r = lane & 31, h = lane >> 5;
  const int row0 = rt * 64, col0 = ct * 72;
  {
    const char* g = ws + WS_WOT + (size_t)col0 * D_ * 2;
    for (int e = wid; e < 81; e += 8) {
      const int pp = e * 64 + lane, row = pp / 72, ch = pp - row * 72;
      glds16(g, (unsigned)(row * 1152 + ((ch ^ (row & 7)) * 16)), lds0 + L2_WO + e * 1024);
    }
  }
  u32x4 aq[4][2][3];
  bf16x8 bk[4][3];
#pragma unroll
  for (int hh = 0; hh < 4; ++hh) {
    const int hd = wid * 4 + hh;
#pragma unroll
    for (int s = 0; s < 3; ++s) {
      bk[hh][s] = *(const bf16x8*)(ws + WS_KVT + ((size_t)(hd * 20 + r) * 48 + 16 * s + 8 * h) * 2);
#pragma unroll
      for (int mt = 0; mt < 2; ++mt)
        aq[hh][mt][s] = *(const u32x4*)(ws + WS_PHIQ + ((size_t)(rt * 2 + mt) * 32 + hd) * 2560 + (s == 2 ? 4 : 2 * s + h) * 512 + r * 16);
    }
  }
  u32x4 pt[2][NCH], xpre[2], bpre[2];
#pragma unroll
  for (int k = 0; k < 2; ++k) {
    const int itx = (k == 0 || tid < 64) ? tid + 512 * k : tid;
    const int row = itx / 9, cg = itx - row * 9;
    const u16* src = (const u16*)(ws + WS_PART) + (size_t)(row0 + row) * D_ + col0 + cg * 8;
#pragma unroll
    for (int cc = 0; cc < NCH; ++cc)
      if (k == 0 || wid == 0) pt[k][cc] = *(const u32x4*)(src + (size_t)cc * S_ * D_);
    if (k == 0 || wid == 0) {
      xpre[k] = *(const u32x4*)(x + (size_t)(row0 + row) * D_ + col0 + cg * 8);
      bpre[k] = *(const u32x4*)(b2 + col0 + cg * 8);
    }
  }
#pragma unroll
  for (int hh = 0; hh < 4; ++hh) {
    const int hd = wid * 4 + hh;
#pragma unroll
    for (int mt = 0; mt < 2; ++mt) {
      f32x16 d16;
#pragma unroll
      for (int i = 0; i < 16; ++i) d16[i] = 0.f;
#pragma unroll
      for (int s = 0; s < 3; ++s) {
        u32x4 av = aq[hh][mt][s];
        if (s == 2) { av[0] = h ? 0u : av[0]; av[1] = h ? 0u : av[1]; av[2] = h ? 0u : av[2]; av[3] = h ? 0u : av[3]; }
        d16 = MFMA32(bk[hh][s], __builtin_bit_cast(bf16x8, av), d16);
      }
      float den = __shfl(d16[10], r);
      den = rbf(rbf(den) + 1e-6f);
      const float rd = __builtin_amdgcn_rcpf(den);
      char* orow = smem + L2_O + (mt * 32 + r) * 1168 + (hd * 18 + 4 * h) * 2;
#pragma unroll
      for (int i = 0; i < 10; i += 2) {
        if (i < 8 || h == 0) {
          float a = rbf(d16[i]), b = rbf(d16[i + 1]);
          float qa = a * rd, qb = b * rd;
          qa = fmaf(fmaf(-qa, den, a), rd, qa); qb = fmaf(fmaf(-qb, den, b), rd, qb);
          *(unsigned*)(orow + ((i & 3) + 8 * (i >> 2)) * 2) = pack2(qa, qb);
        }
      }
    }
  }
  waitv0();
  bar();
  const int mt4 = wid & 3, ng = wid >> 2;
  const int fr = lane & 15, fq = lane >> 4;
  f32x4 sa[3];
#pragma unroll
  for (int n = 0; n < 3; ++n) sa[n] = (f32x4){0.f, 0.f, 0.f, 0.f};
  const int nc0 = ng ? 48 : 0, nc1 = ng ? 56 : 16, nc2 = 32;
  {
    const char* ab = smem + L2_O + (mt4 * 16 + fr) * 1168 + fq * 16;
    const int n0 = nc0 + fr, n1 = nc1 + fr, n2 = nc2 + fr;
    const char* b0e = smem + L2_WO + n0 * 1152 + ((fq ^ (n0 & 7)) * 16);
    const char* b0o = smem + L2_WO + n0 * 1152 + (((4 + fq) ^ (n0 & 7)) * 16);
    const char* b1e = smem + L2_WO + n1 * 1152 + ((fq ^ (n1 & 7)) * 16);
    const char* b1o = smem + L2_WO + n1 * 1152 + (((4 + fq) ^ (n1 & 7)) * 16);
    const char* b2e = smem + L2_WO + n2 * 1152 + ((fq ^ (n2 & 7)) * 16);
    const char* b2o = smem + L2_WO + n2 * 1152 + (((4 + fq) ^ (n2 & 7)) * 16);
#pragma unroll
    for (int ks = 0; ks < 18; ++ks) {
      const int ko = (ks >> 1) * 128;
      bf16x8 av = *(const bf16x8*)(ab + ks * 64);
      sa[0] = MFMA16(av, *(const bf16x8*)(((ks & 1) ? b0o : b0e) + ko), sa[0]);
      sa[1] = MFMA16(av, *(const bf16x8*)(((ks & 1) ? b1o : b1e) + ko), sa[1]);
      sa[2] = MFMA16(av, *(const bf16x8*)(((ks & 1) ? b2o : b2e) + ko), sa[2]);
    }
  }
  bar();
  {
    float* st = (float*)(smem + L2_O);
#pragma unroll
    for (int j = 0; j < 4; ++j) {
      const int row = mt4 * 16 + fq * 4 + j;
      st[row * 76 + nc0 + fr] = rbf(sa[0][j]);
      st[row * 76 + nc1 + fr] = rbf(sa[1][j]);
      if (ng == 0) st[row * 76 + nc2 + fr] = rbf(sa[2][j]);
    }
  }
  bar();
#pragma unroll
  for (int k = 0; k < 2; ++k) {
    if (k == 0 || tid < 64) {
      const int itx = tid + 512 * k;
      const int row = itx / 9, cg = itx - row * 9;
      const f32x4 sv0 = *(const f32x4*)(smem + L2_O + (row * 76 + cg * 8) * 4);
      const f32x4 sv1 = *(const f32x4*)(smem + L2_O + (row * 76 + cg * 8 + 4) * 4);
      const size_t go = (size_t)(row0 + row) * D_ + col0 + cg * 8;
      const u32x4 xv = xpre[k];
      const u32x4 bv = bpre[k];
      u32x4 ov;
#pragma unroll
      for (int j = 0; j < 4; ++j) {
        float f0 = 0.f, f1 = 0.f;
#pragma unroll
        for (int cc = 0; cc < NCH; ++cc) { f0 += lo_bf(pt[k][cc][j]); f1 += hi_bf(pt[k][cc][j]); }
        f0 = rbf(rbf(f0) + lo_bf(bv[j])); f1 = rbf(rbf(f1) + hi_bf(bv[j]));
        const float g0 = rbf(fdiv(f0, 1.0f + __expf(-f0))), g1 = rbf(fdiv(f1, 1.0f + __expf(-f1)));
        const float s0 = j < 2 ? sv0[2 * j] : sv1[2 * j - 4], s1 = j < 2 ? sv0[2 * j + 1] : sv1[2 * j - 3];
        ov[j] = pack2(lo_bf(xv[j]) + rbf(s0 * g0), hi_bf(xv[j]) + rbf(s1 * g1));
      }
      *(u32x4*)(out + go) = ov;
    }
  }
}

extern "C" void kernel_launch(void* const* d_in, const int* in_sizes, int n_in,
                              void* d_out, int out_size, void* d_ws, size_t ws_size,
                              hipStream_t stream) {
  const u16* x = (const u16*)d_in[0];
  const u16* wqkv = (const u16*)d_in[1];
  const u16* wphi = (const u16*)d_in[2];
  const u16* wo = (const u16*)d_in[3];
  const u16* w1 = (const u16*)d_in[5];
  const u16* b1 = (const u16*)d_in[6];
  const u16* w2 = (const u16*)d_in[7];
  const u16* b2 = (const u16*)d_in[8];
  char* ws = (char*)d_ws;
  (void)hipFuncSetAttribute((const void*)k1, hipFuncAttributeMaxDynamicSharedMemorySize, LDS1);
  (void)hipFuncSetAttribute((const void*)k2, hipFuncAttributeMaxDynamicSharedMemorySize, LDS2);
  k0<<<256, 512, 0, stream>>>(x, wqkv, wo, w1, w2, ws);
  k1<<<256, 512, LDS1, stream>>>(x, wqkv, wphi, wo, w1, b1, w2, ws);
  k2<<<256, 512, LDS2, stream>>>(x, b2, ws, (u16*)d_out);
}
```

```cpp
#include <hip/hip_runtime.h>
#include <hip/hip_bf16.h>
#include <stdint.h>
#include <type_traits>


typedef __attribute__((ext_vector_type(8))) __bf16 bf16x8;
typedef __attribute__((ext_vector_type(2))) __bf16 bf16x2;
typedef __attribute__((ext_vector_type(4))) short s16x4;
typedef __attribute__((ext_vector_type(8))) short s16x8;
typedef __attribute__((ext_vector_type(16))) float f32x16;
typedef __attribute__((ext_vector_type(4))) float f32x4;
typedef __attribute__((ext_vector_type(2))) float f32x2;
typedef __attribute__((ext_vector_type(4))) unsigned u32x4;
typedef __attribute__((ext_vector_type(2))) unsigned u32x2;
typedef unsigned short u16;
typedef __attribute__((address_space(3))) s16x4* lds_s16x4_p;

#define DI __device__ __forceinline__

constexpr int S_ = 2048, D_ = 576, F_ = 4608;
constexpr int NCH = 8;
constexpr int FC = F_ / NCH;
constexpr int NMT = S_ / 64;

constexpr size_t WS_CNT  = 0;
constexpr size_t WS_KVP  = 1024;
constexpr size_t WS_KVT  = WS_KVP + (size_t)NMT * 32 * 800 * 4;
constexpr size_t WS_PHIQ = WS_KVT + (size_t)32 * 960 * 2 + 4096;
constexpr size_t WS_WOT  = WS_PHIQ + (size_t)S_ * 32 * 40 * 2 + 256;
constexpr size_t WS_PART = WS_WOT + (size_t)D_ * D_ * 2;
constexpr size_t WS_XN   = WS_PART + (size_t)NCH * S_ * D_ * 2;
constexpr size_t WS_WQ   = WS_XN + (size_t)S_ * D_ * 2;

constexpr int L_W1   = 0;
constexpr int L_W2   = 73728;
constexpr int L_QST  = 0;
constexpr int L_XN   = 86016;
constexpr int L_QT   = 73728;
constexpr int L_PK   = L_QT + 64 * 784;
constexpr int L_X    = 147456;
constexpr int L_B1   = 155648;
constexpr int LDS1   = 163840;

DI float bf2f(u16 v) { return __uint_as_float(((unsigned)v) << 16); }
DI float rbf(float f) { return (float)((__bf16)f); }
DI u16 f2bf(float f) { __bf16 b = (__bf16)f; return __builtin_bit_cast(u16, b); }
DI unsigned pack2(float a, float b) {
  f32x2 v = {a, b};
  bf16x2 p = __builtin_convertvector(v, bf16x2);
  return __builtin_bit_cast(unsigned, p);
}
DI float lo_bf(unsigned u) { return __uint_as_float(u << 16); }
DI float hi_bf(unsigned u) { return __uint_as_float(u & 0xffff0000u); }
DI float fdiv(float a, float b) {
  float r = __builtin_amdgcn_rcpf(b);
  float q = a * r;
  return fmaf(fmaf(-q, b, a), r, q);
}

DI void waitv0()  { asm volatile("s_waitcnt vmcnt(0)" ::: "memory"); }
DI void bar() {
  asm volatile("s_waitcnt lgkmcnt(0)" ::: "memory");
  __builtin_amdgcn_s_barrier();
  asm volatile("" ::: "memory");
}
#define WAITV(n) asm volatile("s_waitcnt vmcnt(" #n ")" ::: "memory")

DI s16x4 tr_read(const char* base, int off) {
  return __builtin_amdgcn_ds_read_tr16_b64_v4i16((lds_s16x4_p)(base + off));
}
DI bf16x8 cat8(s16x4 a, s16x4 b) {
  s16x8 v = __builtin_shufflevector(a, b, 0, 1, 2, 3, 4, 5, 6, 7);
  return __builtin_bit_cast(bf16x8, v);
}
DI void glds16(const void* base, unsigned voff, unsigned lds) {
  unsigned keep;
  asm volatile("s_mov_b32 %0, m0\n\ts_mov_b32 m0, %3\n\ts_nop 0\n\tglobal_load_lds_dwordx4 %1, %2\n\ts_mov_b32 m0, %0"
               : "=&s"(keep) : "v"(voff), "s"(base), "s"(lds) : "memory");
}
#define MFMA32(a, b, c) __builtin_amdgcn_mfma_f32_32x32x16_bf16((a), (b), (c), 0, 0, 0)
#define MFMA16(a, b, c) __builtin_amdgcn_mfma_f32_16x16x32_bf16((a), (b), (c), 0, 0, 0)

DI float gelu_tanh(float x) {
  float t = fmaf(x * x, -0.10294324f, -2.30220820f);
  float e = __builtin_amdgcn_exp2f(x * t);
  return x * __builtin_amdgcn_rcpf(1.0f + e);
}

__global__ __launch_bounds__(512)
void k0(const u16* __restrict__ x, const u16* __restrict__ wqkv, const u16* __restrict__ wo,
        const u16* __restrict__ w1, const u16* __restrict__ w2, char* __restrict__ ws) {
  const int tid = threadIdx.x, lane = tid & 63;
  const int wid = __builtin_amdgcn_readfirstlane(tid >> 6);
  const int b = blockIdx.x;
  if (wid == 0) {
    const int tok = b * 8 + (lane >> 3), part = lane & 7;
    const u16* xr = x + (size_t)tok * D_ + part * 8;
    u32x4 raw[9];
#pragma unroll
    for (int i = 0; i < 9; ++i) raw[i] = *(const u32x4*)(xr + 64 * i);
    float sum = 0.f;
#pragma unroll
    for (int i = 0; i < 9; ++i)
#pragma unroll
      for (int j = 0; j < 4; ++j) sum += lo_bf(raw[i][j]) + hi_bf(raw[i][j]);
    sum += __shfl_xor(sum, 1); sum += __shfl_xor(sum, 2); sum += __shfl_xor(sum, 4);
    const float mean = sum * (1.0f / 576.0f);
    float vs = 0.f;
#pragma unroll
    for (int i = 0; i < 9; ++i)
#pragma unroll
      for (int j = 0; j < 4; ++j) {
        float a = lo_bf(raw[i][j]) - mean, bq = hi_bf(raw[i][j]) - mean;
        vs += a * a + bq * bq;
      }
    vs += __shfl_xor(vs, 1); vs += __shfl_xor(vs, 2); vs += __shfl_xor(vs, 4);
    const float mb = rbf(mean);
    const float vb = rbf(vs * (1.0f / 576.0f));
    const float sd = rbf(sqrtf(rbf(vb + 1e-5f)));
    const float rs = __builtin_amdgcn_rcpf(sd);
    char* dst = ws + WS_XN + (size_t)(tok >> 5) * 36864 + (tok & 31) * 16;
#pragma unroll
    for (int i = 0; i < 9; ++i) {
      u32x4 o;
#pragma unroll
      for (int j = 0; j < 4; ++j) {
        float a = rbf(lo_bf(raw[i][j]) - mb), bq = rbf(hi_bf(raw[i][j]) - mb);
        float qa = a * rs, qb = bq * rs;
        qa = fmaf(fmaf(-qa, sd, a), rs, qa); qb = fmaf(fmaf(-qb, sd, bq), rs, qb);
        o[j] = pack2(qa, qb);
      }
      *(u32x4*)(dst + (part + 8 * i) * 512) = o;
    }
    if (b == 0 && lane < 32) ((u32x4*)(ws + WS_CNT))[lane] = (u32x4){0u, 0u, 0u, 0u};

  } else {
    const int t = tid - 64;
    const int c = b & 7, pr = b >> 3;
    u32x4 q0, q1;
    {
      const int i0 = t, rw0 = i0 / 28, ch0 = min(i0 - rw0 * 28, 26);
      q0 = *(const u32x4*)(wqkv + (size_t)(pr * 18 + rw0) * 1728 + (ch0 / 9) * 576 + c * 72 + (ch0 % 9) * 8);
      const int i1 = min(t + 448, 503), rw1 = i1 / 28, ch1 = min(i1 - rw1 * 28, 26);
      q1 = *(const u32x4*)(wqkv + (size_t)(pr * 18 + rw1) * 1728 + (ch1 / 9) * 576 + c * 72 + (ch1 % 9) * 8);
    }
    u32x4 wv[6];
#pragma unroll
    for (int k = 0; k < 3; ++k) {
      const int i = min(t + 448 * k, 1295), rw = i / 72, ch = i - rw * 72;
      wv[k] = *(const u32x4*)(w1 + (size_t)(pr * 18 + rw) * F_ + c * FC + ch * 8);
      wv[3 + k] = *(const u32x4*)(w2 + (size_t)(c * FC + pr * 18) * D_ + (size_t)i * 8);
    }
    if (b < 72) {
      const int k0r = b * 8;
      for (int n = t; n < 576; n += 448) {
        unsigned o[4];
#pragma unroll
        for (int j = 0; j < 4; ++j)
          o[j] = (unsigned)wo[(size_t)(k0r + 2 * j) * D_ + n] | ((unsigned)wo[(size_t)(k0r + 2 * j + 1) * D_ + n] << 16);
        u32x4 v = {o[0], o[1], o[2], o[3]};
        *(u32x4*)(ws + WS_WOT + ((size_t)n * D_ + k0r) * 2) = v;
      }
    }
    char* qd = ws + WS_WQ + (size_t)c * 258048 + (size_t)pr * 18 * 448;
    *(u32x4*)(qd + t * 16) = q0;
    if (t + 448 < 504) *(u32x4*)(qd + (t + 448) * 16) = q1;
#pragma unroll
    for (int k = 0; k < 6; ++k) asm volatile("" :: "v"(wv[k]));

  }
}

DI void load_wa(const u16* __restrict__ wphi, int head, int r, int h, bf16x8 (&wa)[2]) {
  const int hh = (r >> 2) & 1, ii = (r & 3) + 4 * (r >> 3);
  const u16* wsrc = wphi + (size_t)head * 324 + 9 * hh + ii;
#pragma unroll
  for (int s = 0; s < 2; ++s) {
    s16x8 v;
#pragma unroll
    for (int j = 0; j < 8; ++j) {
      const int d = 16 * s + 8 * h + j;
      v[j] = (ii < 9 && d < 18) ? (short)wsrc[d * 18] : (short)0;
    }
    wa[s] = __builtin_bit_cast(bf16x8, v);
  }
}
template <int QK>
DI void feature_group(char* smem, char* ws, const bf16x8 (&wa)[2], int mt, int hd, int c, int mt64, int r, int h) {
  const char* src = smem + L_QT + (mt * 32 + r) * 784 + (QK * 128 + hd * 32 + 8 * h) * 2;
  bf16x8 b0 = *(const bf16x8*)(src);
  u32x4 b1r = *(const u32x4*)(src + 32);
  b1r[0] = h ? 0u : b1r[0]; b1r[1] = 0u; b1r[2] = 0u; b1r[3] = 0u;
  f32x16 d16;
#pragma unroll
  for (int i = 0; i < 16; ++i) d16[i] = 0.f;
  d16 = MFMA32(wa[0], b0, d16);
  d16 = MFMA32(wa[1], __builtin_bit_cast(bf16x8, b1r), d16);
  float pv[9], mx = -3.0e38f, mn = 3.0e38f;
#pragma unroll
  for (int i = 0; i < 9; ++i) { pv[i] = rbf(d16[i]); mx = fmaxf(mx, pv[i]); mn = fminf(mn, pv[i]); }
  mx = fmaxf(mx, __shfl_xor(mx, 32)); mn = fminf(mn, __shfl_xor(mn, 32));
  float ep[9], en[9], sp = 0.f, sn = 0.f;
#pragma unroll
  for (int i = 0; i < 9; ++i) {
    ep[i] = __expf(pv[i] - mx); sp += ep[i];
    en[i] = __expf(mn - pv[i]); sn += en[i];
  }
  sp += __shfl_xor(sp, 32); sn += __shfl_xor(sn, 32);
  const float rsp = fdiv(1.0f, sp), rsn = fdiv(1.0f, sn);
#pragma unroll
  for (int i = 0; i < 9; ++i) { ep[i] *= rsp; en[i] *= rsn; }
  unsigned o[10];
#pragma unroll
  for (int i = 0; i < 4; ++i) { o[i] = pack2(ep[2 * i], ep[2 * i + 1]); o[5 + i] = pack2(en[2 * i + 1], en[2 * i + 2]); }
  o[4] = pack2(ep[8], en[0]); o[9] = 0u;
  if (QK == 0) {
    char* dst = ws + WS_PHIQ + ((size_t)(mt64 * 2 + mt) * 32 + c * 4 + hd) * 2560 + r * 16;
    if (h == 0) {
      u32x4 v0 = {o[0], o[1], o[2], o[3]}, v1 = {o[4], o[5], o[6], o[7]}; u32x2 v2 = {o[8], o[9]};
      *(u32x4*)(dst) = v0; *(u32x4*)(dst + 512) = v1; *(u32x2*)(dst + 1024) = v2;
    } else {
      u32x2 v0 = {o[0], o[1]}; u32x4 v1 = {o[2], o[3], o[4], o[5]}, v2 = {o[6], o[7], o[8], o[9]};
      *(u32x2*)(dst + 1024 + 8) = v0; *(u32x4*)(dst + 1536) = v1; *(u32x4*)(dst + 2048) = v2;
    }
  } else {
    u32x2* dst = (u32x2*)(smem + L_PK + (hd * 64 + mt * 32 + r) * 88 + h * 40);
#pragma unroll
    for (int i = 0; i < 5; ++i) { u32x2 v = {o[2 * i], o[2 * i + 1]}; dst[i] = v; }
  }
}

__global__ __launch_bounds__(512, 2)
void k1(const u16* __restrict__ x, const u16* __restrict__ wqkv, const u16* __restrict__ wphi,
        const u16* __restrict__ wo, const u16* __restrict__ w1, const u16* __restrict__ b1,
        const u16* __restrict__ w2, char* __restrict__ ws) {
  extern __shared__ __attribute__((aligned(16))) char smem[];
  const unsigned lds0 = (unsigned)(size_t)smem;
  const int tid = threadIdx.x, lane = tid & 63;
  const int wid = __builtin_amdgcn_readfirstlane(tid >> 6);
  const int c = blockIdx.x & 7, mt64 = blockIdx.x >> 3, m0 = mt64 * 64;
  const int r = lane & 31, h = lane >> 5;
  const int blk = (lane >> 4) & 1, q = (lane & 15) >> 2, p = lane & 3;
  auto issue_qkv = [&](int kc) {
    const char* g = ws + WS_WQ + (size_t)c * 258048 + (size_t)kc * 28672;
    const unsigned l = lds0 + L_QST + (kc % 5) * 28672 + (wid - 4) * 7 * 1024;
#pragma unroll
    for (int e = 0; e < 7; ++e) glds16(g, (unsigned)(((wid - 4) * 7 + e) * 1024 + lane * 16), l + e * 1024);
  };
  if (wid >= 4) { issue_qkv(0); issue_qkv(1); }
  if (wid < 4) {
    const int pmt = wid >> 1, pj = wid & 1;
    bf16x8 xn[36];
#pragma unroll
    for (int s = 0; s < 36; ++s)
      xn[s] = *(const bf16x8*)(ws + WS_XN + (size_t)(mt64 * 2 + pmt) * 36864 + s * 1024 + lane * 16);
    {
      f32x16 qa[4];
#pragma unroll
      for (int t = 0; t < 4; ++t)
#pragma unroll
        for (int i = 0; i < 16; ++i) qa[t][i] = 0.f;
      const int lb = (8 * h + q) * 448 + (pj * 4) * 64 + blk * 32 + p * 8;
#pragma unroll
      for (int kc = 0; kc < 9; ++kc) {
        bar();
        int lbk = lb + L_QST + (kc % 5) * 28672; asm volatile("" : "+v"(lbk));
        const char* bb = smem + lbk;
#pragma unroll
        for (int ks = 0; ks < 4; ++ks) {
#pragma unroll
          for (int t = 0; t < 4; ++t) {
            s16x4 a0 = tr_read(bb, ks * 16 * 448 + t * 64);
            s16x4 a1 = tr_read(bb, ks * 16 * 448 + 4 * 448 + t * 64);
            qa[t] = MFMA32(cat8(a0, a1), xn[kc * 4 + ks], qa[t]);
          }
        }
      }
      bar();
      int lz = lane; asm volatile("" : "+v"(lz));
      const int h = lz >> 5;
      char* qt = smem + L_QT + (pmt * 32 + (lz & 31)) * 784;
#pragma unroll
      for (int t = 0; t < 4; ++t) {
#pragma unroll
        for (int g = 0; g < 4; ++g) {
          if (pj == 0 || t < 2 || (t == 2 && g < 3)) {
#pragma unroll
            for (int e = 0; e < 2; ++e) {
              const int u = (pj * 4 + t) * 32 + 8 * g + 4 * h + 2 * e;
              const int pos = u + 14 * ((u * 3641) >> 16);
              *(unsigned*)(qt + pos * 2) = pack2(qa[t][4 * g + 2 * e], qa[t][4 * g + 2 * e + 1]);
            }
          }
        }
      }
    }
    const int e0 = (wid & 3) < 2 ? (wid & 3) * 5 : 10 + ((wid & 3) - 2) * 4;
    const bool five = (wid & 3) < 2;
    unsigned loff[5];
#pragma unroll
    for (int e = 0; e < 5; ++e)
      loff[e] = (unsigned)(((e0 + e) * 8 + (lane >> 3)) * F_ + (((lane & 7) ^ (((lane >> 4) & 1) << 2)) * 8)) * 2u;
    auto issue_w1 = [&](int tau, int e) {
      const u16* g = w1 + (size_t)(tau & 3) * 144 * F_ + c * FC + (tau >> 2) * 64;
      glds16(g, loff[e], lds0 + L_W1 + (tau & 3) * 18432 + (e0 + e) * 1024);
    };
#pragma unroll
    for (int t = 0; t < 4; ++t) {
#pragma unroll
      for (int e = 0; e < 4; ++e) issue_w1(t, e);
      if (five) issue_w1(t, 4);
    }
    if (five) { WAITV(10); } else { WAITV(8); }
    bar();
    {
      bf16x8 wa[2];
      load_wa(wphi, c * 4 + wid, r, h, wa);
#pragma unroll 1
      for (int mt = 0; mt < 2; ++mt) feature_group<0>(smem, ws, wa, mt, wid, c, mt64, r, h);
      waitv0();
    }
    const int lb = (8 * h + q) * 128 + 16 * ((4 * pj + 2 * blk + (p >> 1)) ^ (((q >> 1) & 1) << 2)) + 8 * (p & 1);
    const char* wb = smem + L_W1 + lb;
    char* xdst = smem + L_X + (pmt * 2 + pj) * 2048 + lane * 16;
    f32x16 xa, xp;
#pragma unroll
    for (int i = 0; i < 16; ++i) { xa[i] = 0.f; xp[i] = 0.f; }
    bf16x8 fr[9];
#pragma unroll
    for (int ks = 0; ks < 9; ++ks) fr[ks] = cat8(tr_read(wb, ks * 2048), tr_read(wb, ks * 2048 + 512));
    bar();
    auto iteration = [&](auto DOM, auto DOG, int it, f32x16& xc, const f32x16& xq) {
      constexpr bool doM = decltype(DOM)::value, doG = decltype(DOG)::value;
#pragma unroll
      for (int qd = 0; qd < 4; ++qd) {
        const int tau = it * 4 + qd;
        const bool ld = tau + 4 < 36;
        if (doM && qd == 0) {
          const float* bp = (const float*)(smem + L_B1) + it * 64 + pj * 32 + 4 * h;
#pragma unroll
          for (int g4 = 0; g4 < 4; ++g4) {
            const f32x4 bv = *(const f32x4*)(bp + 8 * g4);
#pragma unroll
            for (int j = 0; j < 4; ++j) xc[4 * g4 + j] = bv[j];
          }
        }
        if (doG && qd == 0) {
          u32x4 v1 = {pack2(xq[8], xq[9]), pack2(xq[10], xq[11]), pack2(xq[12], xq[13]), pack2(xq[14], xq[15])};
          *(u32x4*)(xdst + 1024) = v1;
        }
        if (doG && qd == 1) {
          u32x4 v0;
#pragma unroll
          for (int k = 0; k < 4; ++k) v0[k] = pack2(gelu_tanh(rbf(xq[2 * k])), gelu_tanh(rbf(xq[2 * k + 1])));
          *(u32x4*)(xdst) = v0;
          __builtin_amdgcn_sched_barrier(0);
        }
#pragma unroll
        for (int ks = 0; ks < 9; ++ks) {
          if (doM) {
            const int g = qd * 9 + ks;
            const int noff = ((qd + 1) & 3) * 18432 + ks * 2048;
            xc = MFMA32(fr[ks], xn[g], xc);
            fr[ks] = cat8(tr_read(wb, noff), tr_read(wb, noff + 512));
            if (ld && ks < 4) issue_w1(tau + 4, ks);
          }
        }
        if (doM && ld && five) issue_w1(tau + 4, 4);
        if (!(doM && ld)) { WAITV(0); } else if (five) { WAITV(10); } else { WAITV(8); }
        bar();
      }
    };
    using T_ = std::integral_constant<bool, true>; using F_t = std::integral_constant<bool, false>;
    iteration(T_{}, F_t{}, 0, xa, xp);
#pragma unroll 1
    for (int it = 1; it < 9; it += 2) {
      iteration(T_{}, T_{}, it, xp, xa);
      iteration(T_{}, T_{}, it + 1, xa, xp);
    }
    iteration(F_t{}, T_{}, 9, xp, xa);
    iteration(F_t{}, F_t{}, 10, xp, xa);
    if (wid < 2) {
      unsigned* cnt = (unsigned*)(ws + WS_CNT) + c * 16;
      for (int spin = 0; spin < (1 << 22); ++spin) {
        unsigned v = __hip_atomic_load(cnt, __ATOMIC_RELAXED, __HIP_MEMORY_SCOPE_AGENT);
        if (v >= (unsigned)NMT) break;
        __builtin_amdgcn_s_sleep(2);
      }
      if (tid < 120) {
        const int e = mt64 * 120 + tid;
        const int hd = e / 960, rem = e - hd * 960, d = rem / 48, f = rem - d * 48;
        float s = 0.f;
        if (f < 40) {
          const float* src = (const float*)(ws + WS_KVP) + (size_t)(c * 4 + hd) * 800 + f * 20 + d;
          float v[NMT];
#pragma unroll
          for (int m = 0; m < NMT; ++m)
            asm volatile("global_load_dword %0, %1, off sc1" : "=v"(v[m]) : "v"(src + (size_t)m * 32 * 800) : "memory");
          asm volatile("s_waitcnt vmcnt(0)" ::: "memory");
#pragma unroll
          for (int m = 0; m < NMT; ++m) { asm volatile("" : "+v"(v[m])); s += v[m]; }
        }
        ((u16*)(ws + WS_KVT))[c * 3840 + e] = f2bf(s);
      }
    }
  } else {
    const int cw = wid - 4, hd = cw;
    bf16x8 wa[2];
    load_wa(wphi, c * 4 + hd, r, h, wa);
    WAITV(7);
    issue_qkv(2); issue_qkv(3); issue_qkv(4);
    for (int kc = 0; kc < 9; ++kc) {
      if (kc == 0) { WAITV(28); } else if (kc <= 5) { WAITV(21); } else if (kc == 6) { WAITV(14); } else if (kc == 7) { WAITV(7); } else { WAITV(0); }
      bar();
      if (kc >= 1 && kc + 4 < 9) issue_qkv(kc + 4);
    }
    bar();
    for (int i = tid - 256; i < 576; i += 256) ((float*)(smem + L_B1))[i] = bf2f(b1[c * FC + i]);
    *(u16*)(smem + L_QT + lane * 784 + (256 + hd * 32 + 18) * 2) = 0x3f80;
    bar();
#pragma unroll 1
    for (int mt = 0; mt < 2; ++mt) feature_group<1>(smem, ws, wa, mt, hd, c, mt64, r, h);
    bar();
    const int e0 = cw < 2 ? cw * 5 : 10 + (cw - 2) * 4;
    const bool five = cw < 2;
    unsigned loff[5];
#pragma unroll
    for (int e = 0; e < 5; ++e) {
      int pp = (e0 + e) * 64 + lane, row = pp / 72, ch = pp % 72;
      loff[e] = (unsigned)(row * D_ + ((ch ^ (((row >> 1) & 1) << 2)) * 8)) * 2u;
    }
    auto issue_w2 = [&](int tau, int e) {
      const u16* g = w2 + (size_t)(c * FC + (tau - 8) * 16) * D_;
      glds16(g, loff[e], lds0 + L_W2 + (tau & 3) * 18432 + (e0 + e) * 1024);
    };
    {
      f32x16 kd[2];
#pragma unroll
      for (int t = 0; t < 2; ++t)
#pragma unroll
        for (int i = 0; i < 16; ++i) kd[t][i] = 0.f;
      const char* pa = smem + L_PK + (hd * 64 + 8 * h + q) * 88 + (16 * blk + 4 * p) * 2;
      const char* pb = smem + L_QT + (8 * h + q) * 784 + (256 + hd * 32 + 16 * blk + 4 * p) * 2;
#pragma unroll
      for (int s = 0; s < 4; ++s) {
        bf16x8 bv = cat8(tr_read(pb, s * 16 * 784), tr_read(pb, s * 16 * 784 + 4 * 784));
#pragma unroll
        for (int t = 0; t < 2; ++t) {
          bf16x8 av = cat8(tr_read(pa, s * 16 * 88 + t * 64), tr_read(pa, s * 16 * 88 + 4 * 88 + t * 64));
          kd[t] = MFMA32(av, bv, kd[t]);
        }
      }
      float* kp = (float*)(ws + WS_KVP) + ((size_t)mt64 * 32 + c * 4 + hd) * 800;
      if (r < 20) {
#pragma unroll
        for (int t = 0; t < 2; ++t)
#pragma unroll
          for (int i = 0; i < 16; ++i) {
            int f = t * 32 + (i & 3) + 8 * (i >> 2) + 4 * h;
            if (f < 40) __hip_atomic_store(kp + f * 20 + r, r < 19 ? kd[t][i] : 0.f, __ATOMIC_RELAXED, __HIP_MEMORY_SCOPE_AGENT);
          }
      }
      bar();
      waitv0();
      bar();
    }
    if (tid == 256)
      __hip_atomic_fetch_add((unsigned*)(ws + WS_CNT) + c * 16, 1u, __ATOMIC_RELAXED, __HIP_MEMORY_SCOPE_AGENT);
    waitv0();
    bar(); bar();
    const int cmt = cw >> 1, nh = cw & 1;
    const int sw = (q >> 1) & 1;
    const int rowoff = (4 * h + q) * 1152 + nh * 576 + 16 * (2 * blk + (p >> 1)) + 8 * (p & 1);
    const char* wbA = smem + L_W2 + rowoff + (nh ? -64 * sw : 64 * sw);
    const char* wbB = smem + L_W2 + rowoff + (nh ? 64 * sw : -64 * sw);
    char* xown = smem + L_X + (cmt * 2 + nh) * 2048 + lane * 16;
    auto gelu_frag = [&](u32x4 v) {
      u32x4 o;
#pragma unroll
      for (int k = 0; k < 4; ++k) o[k] = pack2(gelu_tanh(lo_bf(v[k])), gelu_tanh(hi_bf(v[k])));
      return o;
    };
#pragma unroll
    for (int tau = 4; tau < 8; ++tau) {
#pragma unroll
      for (int e = 0; e < 4; ++e) issue_w2(tau + 4, e);
      if (five) issue_w2(tau + 4, 4);
      if (tau < 7) {
        if (tau == 5) *(u32x4*)(xown + 1024) = gelu_frag(*(const u32x4*)(xown + 1024));
        if (five) { WAITV(10); } else { WAITV(8); }
        bar();
      }
    }
    f32x16 acc[9];
#pragma unroll
    for (int n = 0; n < 9; ++n)
#pragma unroll
      for (int i = 0; i < 16; ++i) acc[n][i] = 0.f;
    bf16x8 xf[4], fr[9];
#pragma unroll
    for (int u = 0; u < 4; ++u)
      xf[u] = *(const bf16x8*)(smem + L_X + (cmt * 2 + (u >> 1)) * 2048 + (u & 1) * 1024 + lane * 16);
#pragma unroll
    for (int n = 0; n < 9; ++n) {
      const char* wb = (n & 1) ? wbB : wbA;
      fr[n] = cat8(tr_read(wb, n * 64), tr_read(wb, n * 64 + 8 * 1152));
    }
    if (five) { WAITV(10); } else { WAITV(8); }
    bar();
    for (int it = 2; it < 11; ++it) {
#pragma unroll
      for (int qd = 0; qd < 4; ++qd) {
        const int tau = it * 4 + qd;
        const bool ld = tau + 4 < 44;
        const int nq = ((qd + 1) & 3) * 18432;
        if (qd == 1 && it <= 9) {
          *(u32x4*)(xown + 1024) = gelu_frag(*(const u32x4*)(xown + 1024));
          __builtin_amdgcn_sched_barrier(0);
        }
#pragma unroll
        for (int n = 0; n < 9; ++n) {
          const char* wb = (n & 1) ? wbB : wbA;
          acc[n] = MFMA32(fr[n], xf[qd], acc[n]);
          fr[n] = cat8(tr_read(wb, nq + n * 64), tr_read(wb, nq + n * 64 + 8 * 1152));
          if (ld && n < 4) issue_w2(tau + 4, n);
        }
        if (ld && five) issue_w2(tau + 4, 4);
        if (qd == 3) {
#pragma unroll
          for (int u = 0; u < 4; ++u)
            xf[u] = *(const bf16x8*)(smem + L_X + (cmt * 2 + (u >> 1)) * 2048 + (u & 1) * 1024 + lane * 16);
        }
        if (!ld) { WAITV(0); } else if (five) { WAITV(10); } else { WAITV(8); }
        bar();
      }
    }
    {
      char* img = smem + cw * 18944;
#pragma unroll
      for (int n = 0; n < 9; ++n)
#pragma unroll
        for (int g = 0; g < 4; ++g) {
          u32x2 v = {pack2(acc[n][4 * g], acc[n][4 * g + 1]), pack2(acc[n][4 * g + 2], acc[n][4 * g + 3])};
          *(u32x2*)(img + r * 592 + (n * 32 + 8 * g + 4 * h) * 2) = v;
        }
      asm volatile("s_waitcnt lgkmcnt(0)" ::: "memory");
      u16* pp = (u16*)(ws + WS_PART) + ((size_t)c * S_ + m0 + cmt * 32) * D_ + nh * 288;
#pragma unroll
      for (int i = 0; i < 18; ++i) {
        const int ch = i * 64 + lane, row = ch / 36, cc = ch - row * 36;
        const u32x4 pv = *(const u32x4*)(img + row * 592 + cc * 16);
        *(u32x4*)(pp + (size_t)row * D_ + cc * 8) = pv;
      }
    }
  }
}

constexpr int L2_WO = 0;
constexpr int L2_O  = 82944;
constexpr int LDS2  = L2_O + 64 * 1168;

__global__ __launch_bounds__(512, 2)
void k2(const u16* __restrict__ x, const u16* __restrict__ b2, char* __restrict__ ws, u16* __restrict__ out) {
  extern __shared__ __attribute__((aligned(16))) char smem[];
  const unsigned lds0 = (unsigned)(size_t)smem;
  const int tid = threadIdx.x, lane = tid & 63;
  const int wid = __builtin_amdgcn_readfirstlane(tid >> 6);
  const int rt = (blockIdx.x & 7) * 4 + ((blockIdx.x >> 3) & 3), ct = blockIdx.x >> 5;
  const int r = lane & 31, h = lane >> 5;
  const int row0 = rt * 64, col0 = ct * 72;
  u32x4 aq[4][2][3];
  bf16x8 bk[4][3];
#pragma unroll
  for (int hh = 0; hh < 4; ++hh) {
    const int hd = wid * 4 + hh;
#pragma unroll
    for (int s = 0; s < 3; ++s) {
      bk[hh][s] = *(const bf16x8*)(ws + WS_KVT + ((size_t)(hd * 20 + r) * 48 + 16 * s + 8 * h) * 2);
#pragma unroll
      for (int mt = 0; mt < 2; ++mt)
        aq[hh][mt][s] = *(const u32x4*)(ws + WS_PHIQ + ((size_t)(rt * 2 + mt) * 32 + hd) * 2560 + (s == 2 ? 4 : 2 * s + h) * 512 + r * 16);
    }
  }
  {
    const char* g = ws + WS_WOT + (size_t)col0 * D_ * 2;
    for (int e = wid; e < 81; e += 8) {
      const int pp = e * 64 + lane, row = pp / 72, ch = pp - row * 72;
      glds16(g, (unsigned)(row * 1152 + ((ch ^ (row & 7)) * 16)), lds0 + L2_WO + e * 1024);
    }
  }
  u32x4 pt[2][NCH], xpre[2], bpre[2];
#pragma unroll
  for (int k = 0; k < 2; ++k) {
    const int itx = (k == 0 || tid < 64) ? tid + 512 * k : tid;
    const int row = itx / 9, cg = itx - row * 9;
    const u16* src = (const u16*)(ws + WS_PART) + (size_t)(row0 + row) * D_ + col0 + cg * 8;
#pragma unroll
    for (int cc = 0; cc < NCH; ++cc)
      if (k == 0 || wid == 0) pt[k][cc] = *(const u32x4*)(src + (size_t)cc * S_ * D_);
    if (k == 0 || wid == 0) {
      xpre[k] = *(const u32x4*)(x + (size_t)(row0 + row) * D_ + col0 + cg * 8);
      bpre[k] = *(const u32x4*)(b2 + col0 + cg * 8);
    }
  }
#pragma unroll
  for (int hh = 0; hh < 4; ++hh) {
    const int hd = wid * 4 + hh;
#pragma unroll
    for (int mt = 0; mt < 2; ++mt) {
      f32x16 d16;
#pragma unroll
      for (int i = 0; i < 16; ++i) d16[i] = 0.f;
#pragma unroll
      for (int s = 0; s < 3; ++s) {
        u32x4 av = aq[hh][mt][s];
        if (s == 2) { av[0] = h ? 0u : av[0]; av[1] = h ? 0u : av[1]; av[2] = h ? 0u : av[2]; av[3] = h ? 0u : av[3]; }
        d16 = MFMA32(bk[hh][s], __builtin_bit_cast(bf16x8, av), d16);
      }
      float den = __shfl(d16[10], r);
      den = rbf(rbf(den) + 1e-6f);
      const float rd = __builtin_amdgcn_rcpf(den);
      char* orow = smem + L2_O + (mt * 32 + r) * 1168 + (hd * 18 + 4 * h) * 2;
#pragma unroll
      for (int i = 0; i < 10; i += 2) {
        if (i < 8 || h == 0) {
          float a = rbf(d16[i]), b = rbf(d16[i + 1]);
          float qa = a * rd, qb = b * rd;
          qa = fmaf(fmaf(-qa, den, a), rd, qa); qb = fmaf(fmaf(-qb, den, b), rd, qb);
          *(unsigned*)(orow + ((i & 3) + 8 * (i >> 2)) * 2) = pack2(qa, qb);
        }
      }
    }
  }
  u32x4 gv[2];
#pragma unroll
  for (int k = 0; k < 2; ++k) {
    if (k == 0 || tid < 64) {
#pragma unroll
      for (int j = 0; j < 4; ++j) {
        float f0 = 0.f, f1 = 0.f;
#pragma unroll
        for (int cc = 0; cc < NCH; ++cc) { f0 += lo_bf(pt[k][cc][j]); f1 += hi_bf(pt[k][cc][j]); }
        f0 = rbf(rbf(f0) + lo_bf(bpre[k][j])); f1 = rbf(rbf(f1) + hi_bf(bpre[k][j]));
        gv[k][j] = pack2(fdiv(f0, 1.0f + __expf(-f0)), fdiv(f1, 1.0f + __expf(-f1)));
      }
    }
  }
  waitv0();
  bar();
  const int mt4 = wid & 3, ng = wid >> 2;
  const int fr = lane & 15, fq = lane >> 4;
  f32x4 sa[3];
#pragma unroll
  for (int n = 0; n < 3; ++n) sa[n] = (f32x4){0.f, 0.f, 0.f, 0.f};
  const int nc0 = ng ? 48 : 0, nc1 = ng ? 56 : 16, nc2 = 32;
  {
    const char* ab = smem + L2_O + (mt4 * 16 + fr) * 1168 + fq * 16;
    const int n0 = nc0 + fr, n1 = nc1 + fr, n2 = nc2 + fr;
    const char* b0e = smem + L2_WO + n0 * 1152 + ((fq ^ (n0 & 7)) * 16);
    const char* b0o = smem + L2_WO + n0 * 1152 + (((4 + fq) ^ (n0 & 7)) * 16);
    const char* b1e = smem + L2_WO + n1 * 1152 + ((fq ^ (n1 & 7)) * 16);
    const char* b1o = smem + L2_WO + n1 * 1152 + (((4 + fq) ^ (n1 & 7)) * 16);
    const char* b2e = smem + L2_WO + n2 * 1152 + ((fq ^ (n2 & 7)) * 16);
    const char* b2o = smem + L2_WO + n2 * 1152 + (((4 + fq) ^ (n2 & 7)) * 16);
#pragma unroll
    for (int ks = 0; ks < 18; ++ks) {
      const int ko = (ks >> 1) * 128;
      bf16x8 av = *(const bf16x8*)(ab + ks * 64);
      sa[0] = MFMA16(av, *(const bf16x8*)(((ks & 1) ? b0o : b0e) + ko), sa[0]);
      sa[1] = MFMA16(av, *(const bf16x8*)(((ks & 1) ? b1o : b1e) + ko), sa[1]);
      sa[2] = MFMA16(av, *(const bf16x8*)(((ks & 1) ? b2o : b2e) + ko), sa[2]);
    }
  }
  bar();
  {
    float* st = (float*)(smem + L2_O);
#pragma unroll
    for (int j = 0; j < 4; ++j) {
      const int row = mt4 * 16 + fq * 4 + j;
      st[row * 76 + nc0 + fr] = rbf(sa[0][j]);
      st[row * 76 + nc1 + fr] = rbf(sa[1][j]);
      if (ng == 0) st[row * 76 + nc2 + fr] = rbf(sa[2][j]);
    }
  }
  bar();
#pragma unroll
  for (int k = 0; k < 2; ++k) {
    if (k == 0 || tid < 64) {
      const int itx = tid + 512 * k;
      const int row = itx / 9, cg = itx - row * 9;
      const f32x4 sv0 = *(const f32x4*)(smem + L2_O + (row * 76 + cg * 8) * 4);
      const f32x4 sv1 = *(const f32x4*)(smem + L2_O + (row * 76 + cg * 8 + 4) * 4);
      const size_t go = (size_t)(row0 + row) * D_ + col0 + cg * 8;
      const u32x4 xv = xpre[k];
      u32x4 ov;
#pragma unroll
      for (int j = 0; j < 4; ++j) {
        const float g0 = lo_bf(gv[k][j]), g1 = hi_bf(gv[k][j]);
        const float s0 = j < 2 ? sv0[2 * j] : sv1[2 * j - 4], s1 = j < 2 ? sv0[2 * j + 1] : sv1[2 * j - 3];
        ov[j] = pack2(lo_bf(xv[j]) + rbf(s0 * g0), hi_bf(xv[j]) + rbf(s1 * g1));
      }
      *(u32x4*)(out + go) = ov;
    }
  }
}

extern "C" void kernel_launch(void* const* d_in, const int* in_sizes, int n_in,
                              void* d_out, int out_size, void* d_ws, size_t ws_size,
                              hipStream_t stream) {
  const u16* x = (const u16*)d_in[0];
  const u16* wqkv = (const u16*)d_in[1];
  const u16* wphi = (const u16*)d_in[2];
  const u16* wo = (const u16*)d_in[3];
  const u16* w1 = (const u16*)d_in[5];
  const u16* b1 = (const u16*)d_in[6];
  const u16* w2 = (const u16*)d_in[7];
  const u16* b2 = (const u16*)d_in[8];
  char* ws = (char*)d_ws;
  (void)hipFuncSetAttribute((const void*)k1, hipFuncAttributeMaxDynamicSharedMemorySize, LDS1);
  (void)hipFuncSetAttribute((const void*)k2, hipFuncAttributeMaxDynamicSharedMemorySize, LDS2);
  k0<<<256, 512, 0, stream>>>(x, wqkv, wo, w1, w2, ws);
  k1<<<256, 512, LDS1, stream>>>(x, wqkv, wphi, wo, w1, b1, w2, ws);
  k2<<<256, 512, LDS2, stream>>>(x, b2, ws, (u16*)d_out);
}
```
